```python
import torch

def _softmax(x, axis=-1):
    return torch.softmax(x, dim=axis)
def _gelu(x):
    return torch.nn.functional.gelu(x, approximate="tanh")
def _silu(x):
    return torch.nn.functional.silu(x)
def _sigmoid(x):
    return torch.sigmoid(x)
def _layernorm(x, axis=-1, eps=1e-5):
    m = x.mean(dim=axis, keepdim=True)
    v = x.var(dim=axis, keepdim=True, unbiased=False)
    return (x - m) / torch.sqrt(v + eps)
def _scatter_add(tgt, idx, src, axis=0):
    return tgt.clone().index_add_(axis, idx, src)
def _segment_sum(src, seg, n):
    out = torch.zeros((n,) + src.shape[1:], dtype=src.dtype)
    return out.index_add_(0, seg, src)
def _pad(x, pw):
    flat = tuple(v for pair in reversed(pw) for v in pair)
    return torch.nn.functional.pad(x, flat)

kernel_name = 'sequential_preLN__mha_block_cached__geglu_ffn__decode'

def setup_inputs():

    _w = (lambda a: (lambda t: t.to(torch.bfloat16) if t.is_floating_point() else t)(torch.as_tensor(a)))
    def _A_setup():
        B, Sq, D = 8, 1, 1024
        H, Sk, Dh = 32, 4096, 128
        return {
            "x": _w((lambda *s: torch.randn(*s, dtype=torch.bfloat16) * 0.05)(B, Sq, D)),
            "k_cache": _w((lambda *s: torch.randn(*s, dtype=torch.bfloat16) * 3.2)(B, H, Sk, Dh)),
            "v_cache": _w((lambda *s: torch.randn(*s, dtype=torch.bfloat16) * 0.4)(B, H, Sk, Dh)),
            "wq": _w((lambda *s: torch.randn(*s, dtype=torch.bfloat16) * 3.2)(D, H * Dh) * (D ** -0.5)),
            "wo": _w((lambda *s: torch.randn(*s, dtype=torch.bfloat16) * 0.4)(H * Dh, D) * ((H * Dh) ** -0.5)),
        }
    _A_in = _A_setup()

    def _B_setup():
        B, S, D, F = 8, 1, 1024, 3584
        return {
            "x": _w((lambda *s: torch.randn(*s, dtype=torch.bfloat16) * 0.05)(B, S, D)),
            "w_gate": _w((lambda *s: torch.randn(*s, dtype=torch.bfloat16) * 0.05)(D, F)),
            "w_up": _w((lambda *s: torch.randn(*s, dtype=torch.bfloat16) * 0.05)(D, F)),
            "w_down": _w((lambda *s: torch.randn(*s, dtype=torch.bfloat16) * 0.05)(F, D)),
        }
    _B_in = _B_setup()

    return {
        "A_x": _A_in["x"],
        "A_k_cache": _A_in["k_cache"],
        "A_v_cache": _A_in["v_cache"],
        "A_wq": _A_in["wq"],
        "A_wo": _A_in["wo"],
        "B_x": _B_in["x"],
        "B_w_gate": _B_in["w_gate"],
        "B_w_up": _B_in["w_up"],
        "B_w_down": _B_in["w_down"],
    }

def reference(A_x, A_k_cache, A_v_cache, A_wq, A_wo, B_x, B_w_gate, B_w_up, B_w_down):
    def _A_ref(x, k_cache, v_cache, wq, wo):
        B, Sq, D = x.shape
        H, Dh = 32, 128
        q = (lambda x, *d: x.permute(*d))((x @ wq).reshape(B, Sq, H, Dh), 0, 2, 1, 3)
        scores = torch.einsum("bhqd,bhkd->bhqk", q, k_cache) * (Dh ** -0.5)
        p = _softmax(scores, axis=-1)
        o = torch.einsum("bhqk,bhkd->bhqd", p, v_cache)
        return (lambda x, *d: x.permute(*d))(o, 0, 2, 1, 3).reshape(B, Sq, H * Dh) @ wo

    def _B_ref(x, w_gate, w_up, w_down):
        return (_gelu(x @ w_gate) * (x @ w_up)) @ w_down

    x = A_x
    h = x + _A_ref(_layernorm(x, axis=-1), A_k_cache, A_v_cache, A_wq, A_wo)
    h2 = h + _B_ref(_layernorm(h, axis=-1), B_w_gate, B_w_up, B_w_down)
    return h2
```

```cpp
#include <hip/hip_runtime.h>
#include <hip/hip_bf16.h>
#include <stdint.h>

typedef unsigned short u16;
typedef unsigned v4u __attribute__((ext_vector_type(4)));
typedef float f32x4 __attribute__((ext_vector_type(4)));
typedef short bf16x8 __attribute__((ext_vector_type(8)));
typedef __bf16 bf16x2 __attribute__((ext_vector_type(2)));

#define MIMIC 0

constexpr int B = 8, D = 1024, H = 32, SK = 4096, DH = 128, F = 3584, HD = H * DH;

__device__ __forceinline__ float bf2f(u16 b) { return __builtin_bit_cast(float, (unsigned)b << 16); }
__device__ __forceinline__ float bflo(unsigned u) { return __builtin_bit_cast(float, u << 16); }
__device__ __forceinline__ float bfhi(unsigned u) { return __builtin_bit_cast(float, u & 0xffff0000u); }
__device__ __forceinline__ u16 f2bf(float f) { __bf16 x = (__bf16)f; return __builtin_bit_cast(u16, x); }
__device__ __forceinline__ float rbf(float f) { return bf2f(f2bf(f)); }
__device__ __forceinline__ float mim(float f) { return MIMIC ? rbf(f) : f; }

__device__ __forceinline__ float block_sum(float v, float* red) {
  for (int o = 32; o >= 1; o >>= 1) v += __shfl_xor(v, o);
  const int w = threadIdx.x >> 6, nw = blockDim.x >> 6;
  __syncthreads();
  if ((threadIdx.x & 63) == 0) red[w] = v;
  __syncthreads();
  float s = 0.f;
  for (int i = 0; i < nw; ++i) s += red[i];
  return s;
}

template <bool SRCBF>
__device__ void ln_rows(const void* src, float* xn, float* red) {
  for (int b = 0; b < B; ++b) {
    float v[4];
    for (int i = 0; i < 4; ++i) {
      int c = threadIdx.x + 256 * i;
      v[i] = SRCBF ? bf2f(((const u16*)src)[b * D + c]) : ((const float*)src)[b * D + c];
    }
    float m = block_sum(v[0] + v[1] + v[2] + v[3], red) * (1.f / D);
    float q = 0.f;
    for (int i = 0; i < 4; ++i) q += (v[i] - m) * (v[i] - m);
    float var = block_sum(q, red) * (1.f / D);
    float mm = mim(m), sd = mim(sqrtf(mim(mim(var) + 1e-5f)));
    for (int i = 0; i < 4; ++i) xn[b * D + threadIdx.x + 256 * i] = rbf(mim(v[i] - mm) / sd);
  }
  __syncthreads();
}

__global__ void __launch_bounds__(256) k_q(const u16* x, const u16* wq, float* q) {
  __shared__ float xn[B * D];
  __shared__ float red[8];
  __shared__ float part[16][B][16];
  ln_rows<true>(x, xn, red);
  const int col = blockIdx.x * 16 + (threadIdx.x & 15), kg = threadIdx.x >> 4;
  float acc[B] = {0};
  for (int k = kg * 64; k < kg * 64 + 64; ++k) {
    float w = bf2f(wq[(size_t)k * HD + col]);
    for (int b = 0; b < B; ++b) acc[b] += xn[b * D + k] * w;
  }
  for (int b = 0; b < B; ++b) part[kg][b][threadIdx.x & 15] = acc[b];
  __syncthreads();
  if (threadIdx.x < 128) {
    int b = threadIdx.x >> 4, c = threadIdx.x & 15;
    float s = 0.f;
    for (int g = 0; g < 16; ++g) s += part[g][b][c];
    q[b * HD + blockIdx.x * 16 + c] = rbf(s);
  }
}

template <int CTRL> __device__ __forceinline__ float dpp(float x) {
  return __builtin_bit_cast(float, __builtin_amdgcn_mov_dpp(__builtin_bit_cast(int, x), CTRL, 0xf, 0xf, true));
}
__device__ __forceinline__ float dot2(unsigned k, unsigned q, float acc) {
  return __builtin_amdgcn_fdot2_f32_bf16(__builtin_bit_cast(bf16x2, k), __builtin_bit_cast(bf16x2, q), acc, false);
}
constexpr int XOR1 = 0xB1, XOR2 = 0x4E, XOR7 = 0x141, XOR8 = 0x128;

__global__ void __launch_bounds__(512) k_attn(const float* q, const u16* kc, const u16* vc, float* o) {
  __shared__ float sc[SK];
  __shared__ float red[8];
  __shared__ float oacc[8][4][DH];
  const int bh = blockIdx.x, b = bh / H, h = bh % H;
  const int lane = threadIdx.x & 63, w = threadIdx.x >> 6, g = lane >> 4, c = lane & 15;
  unsigned qp[4];
  for (int i = 0; i < 4; ++i) {
    float a = q[b * HD + h * DH + 8 * c + 2 * i], bb = q[b * HD + h * DH + 8 * c + 2 * i + 1];
    qp[i] = (unsigned)f2bf(a) | ((unsigned)f2bf(bb) << 16);
  }
  const v4u* kb = (const v4u*)(kc + (size_t)bh * SK * DH);
  const v4u* vb = (const v4u*)(vc + (size_t)bh * SK * DH);
  const float scale = MIMIC ? 0.08838834764831845f : 0.08838834764831845f;
  for (int t = w; t < SK / 32; t += 8) {
    v4u kk[8];
#pragma unroll
    for (int i = 0; i < 8; ++i) kk[i] = __builtin_nontemporal_load(kb + (size_t)(t * 32 + i * 4) * 16 + lane);
#pragma unroll
    for (int i = 0; i < 8; ++i) {
      float s = dot2(kk[i].x, qp[0], 0.f); s = dot2(kk[i].y, qp[1], s); s = dot2(kk[i].z, qp[2], s); s = dot2(kk[i].w, qp[3], s);
      s += dpp<XOR8>(s); s += dpp<XOR7>(s); s += dpp<XOR2>(s); s += dpp<XOR1>(s);
      if (c == 0) sc[t * 32 + i * 4 + g] = mim(mim(s) * scale);
    }
  }
  __syncthreads();
  float m = -INFINITY;
  for (int i = threadIdx.x; i < SK; i += 512) m = fmaxf(m, sc[i]);
  for (int o2 = 32; o2 >= 1; o2 >>= 1) m = fmaxf(m, __shfl_xor(m, o2));
  if (lane == 0) red[w] = m;
  __syncthreads();
  m = red[0];
  for (int i = 1; i < 8; ++i) m = fmaxf(m, red[i]);
  float l = 0.f;
  for (int i = threadIdx.x; i < SK; i += 512) { float p = __expf(sc[i] - m); sc[i] = p; l += p; }
  l = block_sum(l, red);
  const float inv = 1.f / l;
  for (int i = threadIdx.x; i < SK; i += 512) sc[i] = mim(sc[i] * inv);
  __syncthreads();
  float acc[8] = {0};
  for (int t = w; t < SK / 32; t += 8) {
    v4u vv[8];
#pragma unroll
    for (int i = 0; i < 8; ++i) vv[i] = __builtin_nontemporal_load(vb + (size_t)(t * 32 + i * 4) * 16 + lane);
#pragma unroll
    for (int i = 0; i < 8; ++i) {
      float p = sc[t * 32 + i * 4 + g];
      acc[0] += p * bflo(vv[i].x); acc[1] += p * bfhi(vv[i].x);
      acc[2] += p * bflo(vv[i].y); acc[3] += p * bfhi(vv[i].y);
      acc[4] += p * bflo(vv[i].z); acc[5] += p * bfhi(vv[i].z);
      acc[6] += p * bflo(vv[i].w); acc[7] += p * bfhi(vv[i].w);
    }
  }
  for (int i = 0; i < 8; ++i) oacc[w][g][8 * c + i] = acc[i];
  __syncthreads();
  if (threadIdx.x < DH) {
    float s = 0.f;
    for (int ww = 0; ww < 8; ++ww) for (int gg = 0; gg < 4; ++gg) s += oacc[ww][gg][threadIdx.x];
    o[b * HD + h * DH + threadIdx.x] = mim(s);
  }
}

__global__ void __launch_bounds__(256) k_wo(const float* o, const u16* wo, const u16* x, float* hout) {
  __shared__ float part[64][B][4];
  const int col = blockIdx.x * 4 + (threadIdx.x & 3), kg = threadIdx.x >> 2;
  float acc[B] = {0};
  for (int k = kg; k < HD; k += 64) {
    float w = bf2f(wo[(size_t)k * D + col]);
    for (int b = 0; b < B; ++b) acc[b] += o[b * HD + k] * w;
  }
  for (int b = 0; b < B; ++b) part[kg][b][threadIdx.x & 3] = acc[b];
  __syncthreads();
  if (threadIdx.x < 32) {
    int b = threadIdx.x >> 2, c = threadIdx.x & 3;
    float s = 0.f;
    for (int g = 0; g < 64; ++g) s += part[g][b][c];
    hout[b * D + blockIdx.x * 4 + c] = mim(bf2f(x[b * D + blockIdx.x * 4 + c]) + mim(s));
  }
}

__device__ __forceinline__ float gelu_tanh(float x) {
  const float k0 = 0.7978845608028654f, k1 = 0.044715f;
  float inner = k0 * (x + k1 * x * x * x);
  return 0.5f * x * (1.f + tanhf(inner));
}

__global__ void __launch_bounds__(256) k_gu(const float* hin, const u16* wg, const u16* wu, float* act) {
  __shared__ float xn[B * D];
  __shared__ float red[8];
  __shared__ float part[16][2][B][16];
  ln_rows<false>(hin, xn, red);
  const int col = blockIdx.x * 16 + (threadIdx.x & 15), kg = threadIdx.x >> 4;
  float ag[B] = {0}, au[B] = {0};
  for (int k = kg * 64; k < kg * 64 + 64; ++k) {
    float w1 = bf2f(wg[(size_t)k * F + col]), w2 = bf2f(wu[(size_t)k * F + col]);
    for (int b = 0; b < B; ++b) { ag[b] += xn[b * D + k] * w1; au[b] += xn[b * D + k] * w2; }
  }
  for (int b = 0; b < B; ++b) { part[kg][0][b][threadIdx.x & 15] = ag[b]; part[kg][1][b][threadIdx.x & 15] = au[b]; }
  __syncthreads();
  if (threadIdx.x < 128) {
    int b = threadIdx.x >> 4, c = threadIdx.x & 15;
    float g = 0.f, u = 0.f;
    for (int i = 0; i < 16; ++i) { g += part[i][0][b][c]; u += part[i][1][b][c]; }
    act[b * F + blockIdx.x * 16 + c] = mim(mim(gelu_tanh(mim(g))) * mim(u));
  }
}

__global__ void __launch_bounds__(256) k_down(const float* act, const u16* wd, const float* hin, u16* out) {
  __shared__ float part[64][B][4];
  const int col = blockIdx.x * 4 + (threadIdx.x & 3), kg = threadIdx.x >> 2;
  float acc[B] = {0};
  for (int k = kg; k < F; k += 64) {
    float w = bf2f(wd[(size_t)k * D + col]);
    for (int b = 0; b < B; ++b) acc[b] += act[b * F + k] * w;
  }
  for (int b = 0; b < B; ++b) part[kg][b][threadIdx.x & 3] = acc[b];
  __syncthreads();
  if (threadIdx.x < 32) {
    int b = threadIdx.x >> 2, c = threadIdx.x & 3;
    float s = 0.f;
    for (int g = 0; g < 64; ++g) s += part[g][b][c];
    out[b * D + blockIdx.x * 4 + c] = f2bf(hin[b * D + blockIdx.x * 4 + c] + mim(s));
  }
}

extern "C" void kernel_launch(void* const* d_in, const int* in_sizes, int n_in, void* d_out, int out_size, void* d_ws,
                              size_t ws_size, hipStream_t stream) {
  const u16* x = (const u16*)d_in[0];
  const u16* kc = (const u16*)d_in[1];
  const u16* vc = (const u16*)d_in[2];
  const u16* wq = (const u16*)d_in[3];
  const u16* wo = (const u16*)d_in[4];
  const u16* wg = (const u16*)d_in[6];
  const u16* wu = (const u16*)d_in[7];
  const u16* wd = (const u16*)d_in[8];
  float* ws = (float*)d_ws;
  float* q = ws; float* o = ws + B * HD; float* h = o + B * HD; float* act = h + B * D;
  k_q<<<256, 256, 0, stream>>>(x, wq, q);
  k_attn<<<256, 512, 0, stream>>>(q, kc, vc, o);
  k_wo<<<256, 256, 0, stream>>>(o, wo, x, h);
  k_gu<<<224, 256, 0, stream>>>(h, wg, wu, act);
  k_down<<<256, 256, 0, stream>>>(act, wd, h, (u16*)d_out);
}
```

```cpp
#define KPOL 2
#define VPOL 2
#include <hip/hip_runtime.h>
#include <hip/hip_bf16.h>
#include <stdint.h>

typedef unsigned short u16;
typedef unsigned v4u __attribute__((ext_vector_type(4)));
typedef float f32x4 __attribute__((ext_vector_type(4)));
typedef short bf16x8 __attribute__((ext_vector_type(8)));
typedef __bf16 bf16x2 __attribute__((ext_vector_type(2)));

#define MIMIC 1

constexpr int B = 8, D = 1024, H = 32, SK = 4096, DH = 128, F = 3584, HD = H * DH;

__device__ __forceinline__ float bf2f(u16 b) { return __builtin_bit_cast(float, (unsigned)b << 16); }
__device__ __forceinline__ float bflo(unsigned u) { return __builtin_bit_cast(float, u << 16); }
__device__ __forceinline__ float bfhi(unsigned u) { return __builtin_bit_cast(float, u & 0xffff0000u); }
__device__ __forceinline__ u16 f2bf(float f) { __bf16 x = (__bf16)f; return __builtin_bit_cast(u16, x); }
__device__ __forceinline__ float rbf(float f) { return bf2f(f2bf(f)); }
__device__ __forceinline__ float mim(float f) { return MIMIC ? rbf(f) : f; }

__device__ __forceinline__ float block_sum(float v, float* red) {
  for (int o = 32; o >= 1; o >>= 1) v += __shfl_xor(v, o);
  const int w = threadIdx.x >> 6, nw = blockDim.x >> 6;
  __syncthreads();
  if ((threadIdx.x & 63) == 0) red[w] = v;
  __syncthreads();
  float s = 0.f;
  for (int i = 0; i < nw; ++i) s += red[i];
  return s;
}

template <bool SRCBF>
__device__ void ln_rows(const void* src, float* xn, float* red) {
  for (int b = 0; b < B; ++b) {
    float v[4];
    for (int i = 0; i < 4; ++i) {
      int c = threadIdx.x + 256 * i;
      v[i] = SRCBF ? bf2f(((const u16*)src)[b * D + c]) : ((const float*)src)[b * D + c];
    }
    float m = block_sum(v[0] + v[1] + v[2] + v[3], red) * (1.f / D);
    float q = 0.f;
    for (int i = 0; i < 4; ++i) q += (v[i] - m) * (v[i] - m);
    float var = block_sum(q, red) * (1.f / D);
    float mm = mim(m), sd = mim(sqrtf(mim(mim(var) + 1e-5f)));
    for (int i = 0; i < 4; ++i) xn[b * D + threadIdx.x + 256 * i] = rbf(mim(v[i] - mm) / sd);
  }
  __syncthreads();
}

__global__ void __launch_bounds__(256) k_q(const u16* x, const u16* wq, float* q) {
  __shared__ float xn[B * D];
  __shared__ float red[8];
  __shared__ float part[16][B][16];
  ln_rows<true>(x, xn, red);
  const int col = blockIdx.x * 16 + (threadIdx.x & 15), kg = threadIdx.x >> 4;
  float acc[B] = {0};
  for (int k = kg * 64; k < kg * 64 + 64; ++k) {
    float w = bf2f(wq[(size_t)k * HD + col]);
    for (int b = 0; b < B; ++b) acc[b] += xn[b * D + k] * w;
  }
  for (int b = 0; b < B; ++b) part[kg][b][threadIdx.x & 15] = acc[b];
  __syncthreads();
  if (threadIdx.x < 128) {
    int b = threadIdx.x >> 4, c = threadIdx.x & 15;
    float s = 0.f;
    for (int g = 0; g < 16; ++g) s += part[g][b][c];
    q[b * HD + blockIdx.x * 16 + c] = rbf(s);
  }
}

template <int CTRL> __device__ __forceinline__ float dpp(float x) {
  return __builtin_bit_cast(float, __builtin_amdgcn_mov_dpp(__builtin_bit_cast(int, x), CTRL, 0xf, 0xf, true));
}
__device__ __forceinline__ float dot2(unsigned k, unsigned q, float acc) {
  return __builtin_amdgcn_fdot2_f32_bf16(__builtin_bit_cast(bf16x2, k), __builtin_bit_cast(bf16x2, q), acc, false);
}
constexpr int XOR1 = 0xB1, XOR2 = 0x4E, XOR7 = 0x141, XOR8 = 0x128;

__global__ void __launch_bounds__(512) k_attn(const float* q, const u16* kc, const u16* vc, float* o) {
  __shared__ float sc[SK];
  __shared__ float red[8];
  __shared__ float oacc[8][4][DH];
  const int bh = blockIdx.x, b = bh / H, h = bh % H;
  const int lane = threadIdx.x & 63, w = threadIdx.x >> 6, g = lane >> 4, c = lane & 15;
  unsigned qp[4];
  for (int i = 0; i < 4; ++i) {
    float a = q[b * HD + h * DH + 8 * c + 2 * i], bb = q[b * HD + h * DH + 8 * c + 2 * i + 1];
    qp[i] = (unsigned)f2bf(a) | ((unsigned)f2bf(bb) << 16);
  }
  const v4u* kb = (const v4u*)(kc + (size_t)bh * SK * DH);
  const v4u* vb = (const v4u*)(vc + (size_t)bh * SK * DH);
  const __amdgpu_buffer_rsrc_t rk = __builtin_amdgcn_make_buffer_rsrc((void*)kb, 0, 1 << 20, 0x00020000);
  const __amdgpu_buffer_rsrc_t rv = __builtin_amdgcn_make_buffer_rsrc((void*)vb, 0, 1 << 20, 0x00020000);
  const float scale = MIMIC ? 0.08838834764831845f : 0.08838834764831845f;
  for (int t = w; t < SK / 32; t += 8) {
    v4u kk[8];
#pragma unroll
    for (int i = 0; i < 8; ++i) kk[i] = __builtin_amdgcn_raw_buffer_load_b128(rk, ((t * 32 + i * 4) * 16 + lane) * 16, 0, KPOL);
#pragma unroll
    for (int i = 0; i < 8; ++i) {
      float s = dot2(kk[i].x, qp[0], 0.f); s = dot2(kk[i].y, qp[1], s); s = dot2(kk[i].z, qp[2], s); s = dot2(kk[i].w, qp[3], s);
      s += dpp<XOR8>(s); s += dpp<XOR7>(s); s += dpp<XOR2>(s); s += dpp<XOR1>(s);
      if (c == 0) sc[t * 32 + i * 4 + g] = mim(mim(s) * scale);
    }
  }
  __syncthreads();
  float m = -INFINITY;
  for (int i = threadIdx.x; i < SK; i += 512) m = fmaxf(m, sc[i]);
  for (int o2 = 32; o2 >= 1; o2 >>= 1) m = fmaxf(m, __shfl_xor(m, o2));
  if (lane == 0) red[w] = m;
  __syncthreads();
  m = red[0];
  for (int i = 1; i < 8; ++i) m = fmaxf(m, red[i]);
  float l = 0.f;
  for (int i = threadIdx.x; i < SK; i += 512) { float p = __expf(sc[i] - m); sc[i] = p; l += p; }
  l = block_sum(l, red);
  const float inv = 1.f / l;
  for (int i = threadIdx.x; i < SK; i += 512) sc[i] = mim(sc[i] * inv);
  __syncthreads();
  float acc[8] = {0};
  for (int t = w; t < SK / 32; t += 8) {
    v4u vv[8];
#pragma unroll
    for (int i = 0; i < 8; ++i) vv[i] = __builtin_amdgcn_raw_buffer_load_b128(rv, ((t * 32 + i * 4) * 16 + lane) * 16, 0, VPOL);
#pragma unroll
    for (int i = 0; i < 8; ++i) {
      float p = sc[t * 32 + i * 4 + g];
      acc[0] += p * bflo(vv[i].x); acc[1] += p * bfhi(vv[i].x);
      acc[2] += p * bflo(vv[i].y); acc[3] += p * bfhi(vv[i].y);
      acc[4] += p * bflo(vv[i].z); acc[5] += p * bfhi(vv[i].z);
      acc[6] += p * bflo(vv[i].w); acc[7] += p * bfhi(vv[i].w);
    }
  }
  for (int i = 0; i < 8; ++i) oacc[w][g][8 * c + i] = acc[i];
  __syncthreads();
  if (threadIdx.x < DH) {
    float s = 0.f;
    for (int ww = 0; ww < 8; ++ww) for (int gg = 0; gg < 4; ++gg) s += oacc[ww][gg][threadIdx.x];
    o[b * HD + h * DH + threadIdx.x] = mim(s);
  }
}

__global__ void __launch_bounds__(256) k_wo(const float* o, const u16* wo, const u16* x, float* hout) {
  __shared__ float part[64][B][4];
  const int col = blockIdx.x * 4 + (threadIdx.x & 3), kg = threadIdx.x >> 2;
  float acc[B] = {0};
  for (int k = kg; k < HD; k += 64) {
    float w = bf2f(wo[(size_t)k * D + col]);
    for (int b = 0; b < B; ++b) acc[b] += o[b * HD + k] * w;
  }
  for (int b = 0; b < B; ++b) part[kg][b][threadIdx.x & 3] = acc[b];
  __syncthreads();
  if (threadIdx.x < 32) {
    int b = threadIdx.x >> 2, c = threadIdx.x & 3;
    float s = 0.f;
    for (int g = 0; g < 64; ++g) s += part[g][b][c];
    hout[b * D + blockIdx.x * 4 + c] = mim(bf2f(x[b * D + blockIdx.x * 4 + c]) + mim(s));
  }
}

__device__ __forceinline__ float gelu_tanh(float x) {
  const float k0 = 0.7978845608028654f, k1 = 0.044715f;
  float inner = k0 * (x + k1 * x * x * x);
  return 0.5f * x * (1.f + tanhf(inner));
}

__global__ void __launch_bounds__(256) k_gu(const float* hin, const u16* wg, const u16* wu, float* act) {
  __shared__ float xn[B * D];
  __shared__ float red[8];
  __shared__ float part[16][2][B][16];
  ln_rows<false>(hin, xn, red);
  const int col = blockIdx.x * 16 + (threadIdx.x & 15), kg = threadIdx.x >> 4;
  float ag[B] = {0}, au[B] = {0};
  for (int k = kg * 64; k < kg * 64 + 64; ++k) {
    float w1 = bf2f(wg[(size_t)k * F + col]), w2 = bf2f(wu[(size_t)k * F + col]);
    for (int b = 0; b < B; ++b) { ag[b] += xn[b * D + k] * w1; au[b] += xn[b * D + k] * w2; }
  }
  for (int b = 0; b < B; ++b) { part[kg][0][b][threadIdx.x & 15] = ag[b]; part[kg][1][b][threadIdx.x & 15] = au[b]; }
  __syncthreads();
  if (threadIdx.x < 128) {
    int b = threadIdx.x >> 4, c = threadIdx.x & 15;
    float g = 0.f, u = 0.f;
    for (int i = 0; i < 16; ++i) { g += part[i][0][b][c]; u += part[i][1][b][c]; }
    act[b * F + blockIdx.x * 16 + c] = mim(mim(gelu_tanh(mim(g))) * mim(u));
  }
}

__global__ void __launch_bounds__(256) k_down(const float* act, const u16* wd, const float* hin, u16* out) {
  __shared__ float part[64][B][4];
  const int col = blockIdx.x * 4 + (threadIdx.x & 3), kg = threadIdx.x >> 2;
  float acc[B] = {0};
  for (int k = kg; k < F; k += 64) {
    float w = bf2f(wd[(size_t)k * D + col]);
    for (int b = 0; b < B; ++b) acc[b] += act[b * F + k] * w;
  }
  for (int b = 0; b < B; ++b) part[kg][b][threadIdx.x & 3] = acc[b];
  __syncthreads();
  if (threadIdx.x < 32) {
    int b = threadIdx.x >> 2, c = threadIdx.x & 3;
    float s = 0.f;
    for (int g = 0; g < 64; ++g) s += part[g][b][c];
    out[b * D + blockIdx.x * 4 + c] = f2bf(hin[b * D + blockIdx.x * 4 + c] + mim(s));
  }
}

extern "C" void kernel_launch(void* const* d_in, const int* in_sizes, int n_in, void* d_out, int out_size, void* d_ws,
                              size_t ws_size, hipStream_t stream) {
  const u16* x = (const u16*)d_in[0];
  const u16* kc = (const u16*)d_in[1];
  const u16* vc = (const u16*)d_in[2];
  const u16* wq = (const u16*)d_in[3];
  const u16* wo = (const u16*)d_in[4];
  const u16* wg = (const u16*)d_in[6];
  const u16* wu = (const u16*)d_in[7];
  const u16* wd = (const u16*)d_in[8];
  float* ws = (float*)d_ws;
  float* q = ws; float* o = ws + B * HD; float* h = o + B * HD; float* act = h + B * D;
  k_q<<<256, 256, 0, stream>>>(x, wq, q);
  k_attn<<<256, 512, 0, stream>>>(q, kc, vc, o);
  k_wo<<<256, 256, 0, stream>>>(o, wo, x, h);
  k_gu<<<224, 256, 0, stream>>>(h, wg, wu, act);
  k_down<<<256, 256, 0, stream>>>(act, wd, h, (u16*)d_out);
}
```

```cpp
#include <hip/hip_runtime.h>
#include <hip/hip_bf16.h>
#include <stdint.h>

#define PRE 32
#define LV0 6
#define PER 3
#define KDN 2.5f
#define KUP 5.0f
#define LWIN 6
#define GPRE 4
#define QNAP 32
#define NAP4 14
#define NAP5 14
#define CPF 20
#define WOAT 36
#define WOWIN 4
#define NAP1 8
#define WNT 0

namespace {
typedef unsigned short u16;
typedef unsigned long long u64;
typedef unsigned v4u __attribute__((ext_vector_type(4)));
typedef unsigned v2u __attribute__((ext_vector_type(2)));
typedef float f32x4 __attribute__((ext_vector_type(4)));
typedef float f32x2 __attribute__((ext_vector_type(2)));
typedef short bf16x8 __attribute__((ext_vector_type(8)));
typedef __bf16 bf16x2 __attribute__((ext_vector_type(2)));
typedef __amdgpu_buffer_rsrc_t rsrc_t;
#define GAS __attribute__((address_space(1)))
#define LAS __attribute__((address_space(3)))
#define RLX_AGENT __ATOMIC_RELAXED, __HIP_MEMORY_SCOPE_AGENT
#define RLX_WG __ATOMIC_RELAXED, __HIP_MEMORY_SCOPE_WORKGROUP
template <int N> using ic = std::integral_constant<int, N>;

constexpr int B = 8, D = 1024, H = 32, SK = 4096, DH = 128, F = 3584, HD = H * DH;
constexpr int NWG = 256, NTHR = 512, DEPTH = 6;
constexpr int AUX_NT = 2, AUX_SC1 = 16, RSRC3 = 0x00020000;
constexpr int WS_EPOCH = 0, WS_FLAGS = 2048;
constexpr int WS_O = 4096;
constexpr int WS_H = WS_O + B * HD * 2;
constexpr int WS_P1 = WS_H + B * D * 2;
constexpr int WS_GP = WS_P1 + 128 * H * 256;
constexpr int WS_P2 = WS_GP + 28 * 4 * 4096;
constexpr int WS_QX = WS_P2 + 128 * 28 * 256;
constexpr int WS_BRD = WS_QX + NWG * 4096;
constexpr int WS_END = WS_BRD + 16 * 1024;
constexpr int L_WO = 0, L_GU = 32768, L_DN = 98304, L_RED = 131072, L_XN = L_RED + 16384, L_QP = L_XN + 4096, L_PART = L_QP + 2048,
              L_STAT = L_PART + 2080, L_CTL = L_STAT + 512, L_END = L_CTL + 256;
enum { C_PROG, C_BOOT, C_ARR, C_XN, C_SM, C_WO, C_OG, C_GU, C_DN, C_LEVEL, C_RANK = 32 };
constexpr unsigned SENT = 0xFFFFFFFFu;
constexpr unsigned SPIN_TICKS = 2000000;

__device__ __forceinline__ float bflo(unsigned u) { return __builtin_bit_cast(float, u << 16); }
__device__ __forceinline__ float bfhi(unsigned u) { return __builtin_bit_cast(float, u & 0xffff0000u); }
__device__ __forceinline__ unsigned pk(float lo, float hi) {
  bf16x2 v = {(__bf16)lo, (__bf16)hi};
  return __builtin_bit_cast(unsigned, v);
}
__device__ __forceinline__ float rbf(float f) { return bflo(pk(f, f)); }
__device__ __forceinline__ float divr(float a, float d, float r) { const float q = a * r; return fmaf(fmaf(-q, d, a), r, q); }
__device__ __forceinline__ unsigned nosent(unsigned u) { return u == SENT ? 0x7FC07FC0u : u; }
__device__ __forceinline__ v4u tagged(v4u u, unsigned tag) {
  v4u r;
#pragma unroll
  for (int i = 0; i < 4; ++i) r[i] = ((u[i] + 0x80u) & 0xFFFFFF00u) | ((tag >> (8 * i)) & 0xFFu);
  return r;
}
__device__ __forceinline__ unsigned tagdiff(v4u u, unsigned tag) {
  return (__builtin_amdgcn_perm(u.y, u.x, 0x0c0c0400u) | __builtin_amdgcn_perm(u.w, u.z, 0x04000c0cu)) ^ tag;
}
__device__ __forceinline__ f32x4 untag(v4u u) { return __builtin_bit_cast(f32x4, u & 0xFFFFFF00u); }
template <int CT> __device__ __forceinline__ float dpp(float x) {
  return __builtin_bit_cast(float, __builtin_amdgcn_mov_dpp(__builtin_bit_cast(int, x), CT, 0xf, 0xf, true));
}
template <int CT> __device__ __forceinline__ unsigned dppu(unsigned x) { return (unsigned)__builtin_amdgcn_mov_dpp((int)x, CT, 0xf, 0xf, true); }
constexpr int XOR1 = 0xB1, XOR2 = 0x4E, XOR7 = 0x141, XOR8 = 0x128, BCAST = 0x150;
__device__ __forceinline__ float dot2(unsigned k, unsigned q, float acc) {
  return __builtin_amdgcn_fdot2_f32_bf16(__builtin_bit_cast(bf16x2, k), __builtin_bit_cast(bf16x2, q), acc, false);
}
__device__ __forceinline__ float xrow_max(float x) {
  auto s = __builtin_amdgcn_permlane16_swap(__float_as_uint(x), __float_as_uint(x), false, false);
  x = fmaxf(__uint_as_float(s[0]), __uint_as_float(s[1]));
  auto t = __builtin_amdgcn_permlane32_swap(__float_as_uint(x), __float_as_uint(x), false, false);
  return fmaxf(__uint_as_float(t[0]), __uint_as_float(t[1]));
}
__device__ __forceinline__ float xrow_sum(float x) {
  auto s = __builtin_amdgcn_permlane16_swap(__float_as_uint(x), __float_as_uint(x), false, false);
  x = __uint_as_float(s[0]) + __uint_as_float(s[1]);
  auto t = __builtin_amdgcn_permlane32_swap(__float_as_uint(x), __float_as_uint(x), false, false);
  return __uint_as_float(t[0]) + __uint_as_float(t[1]);
}
__device__ __forceinline__ float wave_max(float x) {
  x = fmaxf(x, dpp<XOR1>(x)); x = fmaxf(x, dpp<XOR2>(x)); x = fmaxf(x, dpp<XOR7>(x)); x = fmaxf(x, dpp<XOR8>(x));
  return xrow_max(x);
}
__device__ __forceinline__ float wave_sum(float x) {
  x += dpp<XOR1>(x); x += dpp<XOR2>(x); x += dpp<XOR7>(x); x += dpp<XOR8>(x);
  return xrow_sum(x);
}
__device__ __forceinline__ f32x4 mfma(v4u a, v4u b, f32x4 c) {
  return __builtin_amdgcn_mfma_f32_16x16x32_bf16(__builtin_bit_cast(bf16x8, a), __builtin_bit_cast(bf16x8, b), c, 0, 0, 0);
}
#define LD128(r, voff, soff, aux) __builtin_amdgcn_raw_buffer_load_b128((r), (voff), (soff), (aux))
#define ST128(v, r, voff) __builtin_amdgcn_raw_buffer_store_b128((v), (r), (voff), 0, AUX_SC1)
#define SCHED() __builtin_amdgcn_sched_barrier(0)
#define BARRIER() do { SCHED(); asm volatile("s_waitcnt lgkmcnt(0)\n\ts_barrier" ::: "memory"); SCHED(); } while (0)
#define DRAIN() asm volatile("s_waitcnt vmcnt(0)" ::: "memory")
#define VMWAIT(n) asm volatile("s_waitcnt vmcnt(%0)" ::"i"(n))
#define PIN(x) asm volatile("" : "+v"(x))
__device__ __forceinline__ unsigned now() { return (unsigned)__builtin_amdgcn_s_memrealtime(); }
__device__ __forceinline__ rsrc_t rsrc(const void* p, int bytes) { return __builtin_amdgcn_make_buffer_rsrc((void*)p, 0, bytes, RSRC3); }
__device__ __forceinline__ v4u ror8(v4u a) { return v4u{dppu<XOR8>(a.x), dppu<XOR8>(a.y), dppu<XOR8>(a.z), dppu<XOR8>(a.w)}; }
__device__ __forceinline__ unsigned umax4(v4u a) { return max(max(a.x, a.y), max(a.z, a.w)); }
__device__ __forceinline__ unsigned bperm(int src, float v) { return __builtin_amdgcn_ds_bpermute(src * 4, __float_as_uint(v)); }
__device__ __forceinline__ v4u trfrag(LAS unsigned char* p, int off2) {
  typedef short v4i16_t __attribute__((ext_vector_type(4)));
  v2u lo = __builtin_bit_cast(v2u, __builtin_amdgcn_ds_read_tr16_b64_v4i16((LAS v4i16_t*)p));
  v2u hi = __builtin_bit_cast(v2u, __builtin_amdgcn_ds_read_tr16_b64_v4i16((LAS v4i16_t*)(p + off2)));
  return v4u{lo.x, lo.y, hi.x, hi.y};
}
__device__ __forceinline__ f32x4 rowmac(v4u a, unsigned pb, v4u msk, f32x4 acc) { return mfma(a, v4u{pb & msk.x, pb & msk.y, pb & msk.z, pb & msk.w}, acc); }
template <int N, class Fn> __device__ __forceinline__ void unroll(Fn&& f) {
  if constexpr (N > 0) { unroll<N - 1>(f); f(ic<N - 1>{}); }
}
__device__ __forceinline__ float transpose_reduce(float (&x)[16], int c) {
#define TR_STEP(HALF, CT, BIT) _Pragma("unroll") for (int i = 0; i < (HALF); ++i) { \
    const float lo_ = x[i], hi_ = x[i + (HALF)]; const float t1_ = lo_ + dpp<CT>(lo_), t2_ = hi_ + dpp<CT>(hi_); x[i] = (c & (BIT)) ? t2_ : t1_; }
  TR_STEP(8, XOR8, 8) TR_STEP(4, XOR7, 4) TR_STEP(2, XOR2, 2) TR_STEP(1, XOR1, 1)
#undef TR_STEP
  return x[0];
}
constexpr int tile_off(int i) { return ((i / 4) * 16 + (i % 4)) * 1024; }
__device__ __forceinline__ void dma(rsrc_t rs, unsigned lds, int voff, int soff) {
  unsigned keep;
  asm volatile("s_mov_b32 %0, m0\n\ts_mov_b32 m0, %1\n\ts_nop 4\n\tbuffer_load_dwordx4 %2, %3, %4 offen lds\n\ts_mov_b32 m0, %0"
               : "=&s"(keep) : "s"(lds), "v"(voff), "s"(rs), "s"(soff) : "memory");
}
__device__ __forceinline__ void gate(int mode) {
  asm volatile("s_cmp_gt_u32 %0, 3\n\ts_cbranch_scc1 1f\n\ts_waitcnt vmcnt(6)\n\ts_cmp_gt_u32 %0, 2\n\ts_cbranch_scc1 1f\n\ts_waitcnt vmcnt(5)\n\t"
               "s_cmp_gt_u32 %0, 1\n\ts_cbranch_scc1 1f\n\ts_waitcnt vmcnt(4)\n\ts_cmp_gt_u32 %0, 0\n\ts_cbranch_scc1 1f\n\ts_waitcnt vmcnt(3)\n1:"
               ::"s"(mode) : "scc");
}
#define TIMED_OUT(it) (((it) & 63u) == 0u && now() - t0 > SPIN_TICKS)
template <int N> __device__ __forceinline__ void gather_tagged(v4u (&a)[N], rsrc_t rs, int voff, unsigned tag, unsigned t0) {
  unsigned todo = (1u << N) - 1u;
#pragma unroll
  for (int u = 0; u < N; ++u) a[u] = v4u{0u, 0u, 0u, 0u};
  for (unsigned it = 1;; ++it) {
#pragma unroll
    for (int u = 0; u < N; ++u) if (todo & (1u << u)) a[u] = LD128(rs, voff, u * 1024, AUX_SC1);
#pragma unroll
    for (int u = 0; u < N; ++u) if (__builtin_amdgcn_ballot_w64(tagdiff(a[u], tag) != 0u) == 0ull) todo &= ~(1u << u);
    if (todo == 0u || TIMED_OUT(it)) break;
  }
}

template <int N> __device__ __forceinline__ void gather_all(v4u (&a)[N], rsrc_t rs, int voff, int stride, unsigned tag, bool skip, unsigned t0) {
  for (unsigned it = 1;; ++it) {
#pragma unroll
    for (int u = 0; u < N; ++u) a[u] = LD128(rs, (u == N - 1 && skip) ? WS_END : voff, u * stride, AUX_SC1);
    unsigned m = skip ? 0u : tagdiff(a[N - 1], tag);
#pragma unroll
    for (int u = 0; u < N - 1; ++u) m |= tagdiff(a[u], tag);
    if (__builtin_amdgcn_ballot_w64(m != 0u) == 0ull || TIMED_OUT(it)) break;
  }
}

struct Params {
  const u16 *x, *kc, *vc, *wq, *wo, *wg, *wu, *wd;
  u16* out;
  unsigned char* ws;
};

__global__ void __launch_bounds__(NTHR, 2) fused_decode_block(Params p) {
  __shared__ __attribute__((aligned(16))) unsigned char smem[L_END];
  LAS unsigned char* L = (LAS unsigned char*)smem;
  volatile LAS unsigned* ctl = (volatile LAS unsigned*)(L + L_CTL);
  const unsigned lds0 = (unsigned)(size_t)L;
  const int tid = threadIdx.x, lane = tid & 63, w = __builtin_amdgcn_readfirstlane(tid >> 6), hw = w & 3;
  const int g = lane >> 4, c = lane & 15, rb = c & 7, up = c >> 3;
  const int bh = blockIdx.x, b = bh >> 5, h = bh & 31;
  const unsigned t0 = now();
  const rsrc_t rws = rsrc(p.ws, WS_END);
  auto wait_lds = [&](int word, unsigned want, auto NAP) {
    for (unsigned it = 1; ctl[word] < want; ++it) {
      if (NAP) __builtin_amdgcn_s_sleep(decltype(NAP)::value);
      if (TIMED_OUT(it)) break;
    }
    asm volatile("" ::: "memory");
  };
  auto arrive = [&](int word) {
    asm volatile("s_waitcnt lgkmcnt(0)" ::: "memory");
    if (lane == 0) __hip_atomic_fetch_add((LAS unsigned*)(L + L_CTL) + word, 1u, RLX_WG);
  };
  const int r8 = lane >> 3, nar_src = (((lane >> 1) & 3) ^ ((r8 >> 1) & 1)) << 5 | (lane & 1) << 4;
  const int q4 = (lane >> 2) & 3, nar_f = (q4 >> 1) | ((g & 1) << 1);

  const rsrc_t rk = rsrc(p.kc + (size_t)bh * SK * DH, SK * DH * 2), rx = rsrc(p.x, B * D * 2);
  const int vo = hw * 4096 + lane * 16;
  v4u k0[16], xr[4], xs;
  u64 epoch;
  unsigned code;
  if (w < 4) {
#pragma unroll
    for (int i = 0; i < DEPTH; ++i) k0[i] = LD128(rk, vo, tile_off(i), AUX_NT);
    SCHED();
  } else {
    u64 pc;
    asm volatile("s_getpc_b64 %0" : "=s"(pc));
    code = __builtin_amdgcn_raw_buffer_load_b32(rsrc((const void*)(pc & ~255ull), CPF * 1024), (((bh >> 3) & 7) * 4 + hw) * 1024 + (lane & 7) * 128, 0, 0);
    SCHED();
#pragma unroll
    for (int i = 0; i < 4; ++i) xr[i] = LD128(rx, vo, i * 1024, 0);
    xs = LD128(rx, (2 * hw + (lane >> 5)) * 2048 + (b >> 1) * 512 + (lane & 31) * 16, 0, 0);
    epoch = __hip_atomic_load((GAS u64*)(p.ws + WS_EPOCH), RLX_AGENT);
    SCHED();
    const rsrc_t rw = rsrc(p.wq, D * HD * 2);
    const int qv = ((b >> 1) * 256 + hw * 64 + r8) * (HD * 2) + h * 256 + (b & 1) * 128;
#pragma unroll
    for (int i = 0; i < 8; ++i)
      __builtin_amdgcn_raw_ptr_buffer_load_lds(rw, (LAS void*)(L + L_GU + (hw * 8 + i) * 1024), 16, qv + (nar_src ^ ((i & 1) << 6)), i * 8 * HD * 2, 0, WNT ? AUX_NT : 0);
    if (w == 4) ctl[lane] = lane == C_LEVEL ? LV0 : 0u;
  }
  BARRIER();

  constexpr float SCALE = 0.08838834764831845f, LOG2E = 1.4426950408889634f;
  const int xl = bh & 7, xi = bh >> 3, n = xl * 28 + xi;
  const bool ffn = xi < 28;
  const int gm = n / 112, gcb = (n >> 1) % 56, gkh = n & 1;
  const int dhg = n >> 3, dnb = n & 7;
  auto load_sq = [&](const u16* base, int rows, int row0, int col0, int lds, auto WIN) {
    const rsrc_t rw = rsrc(base, rows * D * 2);
    const int wv = (row0 + g) * (D * 2) + col0 * 2 + (c & 1) * 16;
#pragma unroll 1
    for (int i = hw * 8; i < hw * 8 + 8; ++i) {
      VMWAIT(WIN - 1);
      dma(rw, lds0 + lds + i * 1024, wv + (((c >> 1) ^ g ^ ((i << 1) & 4)) << 5), i * 4 * D * 2);
    }
  };

  if (w < 4) {
    const rsrc_t rv = rsrc(p.vc + (size_t)bh * SK * DH, SK * DH * 2);
    const unsigned half = up ? 0u : (c & 1) ? 0xFFFF0000u : 0x0000FFFFu;
    const v4u msk = {rb >> 1 == 0 ? half : 0u, rb >> 1 == 1 ? half : 0u, rb >> 1 == 2 ? half : 0u, rb >> 1 == 3 ? half : 0u};
    v4u k1[16], r[8];
    auto first = [&](auto I) {
      constexpr int i = I;
      VMWAIT(DEPTH - 1);
      if constexpr (i < 16) k0[i] = LD128(rk, vo, tile_off(i), AUX_NT);
      else if constexpr (i < 32) k1[i - 16] = LD128(rk, vo, 65536 + tile_off(i - 16), AUX_NT);
      else r[i - 32] = LD128(rk, vo, 131072 + tile_off(i - 32), AUX_NT);
      SCHED();
    };
    unroll<PRE - DEPTH>([&](auto J) { first(ic<DEPTH + J>{}); });
    BARRIER();
    auto rest = [&](auto STEP) {
      constexpr int lo = PRE + (40 - PRE) * STEP / 8, hi = PRE + (40 - PRE) * (STEP + 1) / 8;
      SCHED();
      unroll<hi - lo>([&](auto I) { first(ic<lo + I>{}); });
    };
    static_assert(PRE >= 32, "tiles 0 and 1 are requested before q is waited for");
    unsigned q[4];
    {
      const LAS f32x4* qp = (const LAS f32x4*)(L + L_QP);
      const f32x4 lo = (qp[2 * c] + qp[32 + 2 * c]) + (qp[64 + 2 * c] + qp[96 + 2 * c]);
      const f32x4 hi = (qp[2 * c + 1] + qp[33 + 2 * c]) + (qp[65 + 2 * c] + qp[97 + 2 * c]);
      q[0] = pk(lo.x, lo.y); q[1] = pk(lo.z, lo.w); q[2] = pk(hi.x, hi.y); q[3] = pk(hi.z, hi.w);
    }
    LAS float* sct = (LAS float*)(L + L_RED) + w * 1024 + lane;
    auto qk = [&](v4u x) { return dot2(x.w, q[3], dot2(x.z, q[2], dot2(x.y, q[1], dot2(x.x, q[0], 0.f)))); };
    float mx;
    {
      float s[16];
      unroll<16>([&](auto I) { if constexpr (I % 4 == 0) rest(ic<I / 4>{}); s[I] = qk(k0[I]); });
      mx = rbf(rbf(transpose_reduce(s, c)) * SCALE);
      sct[0] = mx;
      unroll<16>([&](auto I) { if constexpr (I % 4 == 0) rest(ic<4 + I / 4>{}); s[I] = qk(k1[I]); });
      const float sc = rbf(rbf(transpose_reduce(s, c)) * SCALE);
      sct[64] = sc;
      mx = fmaxf(mx, sc);
    }
    SCHED();
    int md[4];
    auto level = [&](int t) {
      unsigned rank = 0;
      if (lane == 0) rank = __hip_atomic_fetch_add((LAS unsigned*)(L + L_CTL) + C_RANK + t, 1u, RLX_WG);
      int lv = ctl[C_LEVEL];
      rank = __builtin_amdgcn_readfirstlane(rank);
      if (rank == 1u) ctl[C_PROG] = t;
      lv = __builtin_amdgcn_readfirstlane(lv) + (rank == 3u) - (rank == 0u);
      lv = __builtin_amdgcn_readfirstlane(lv < 0 ? 0 : lv > 16 ? 16 : lv);
      md[0] = lv >> 2; md[1] = (lv + 2) >> 2; md[2] = (lv + 1) >> 2; md[3] = (lv + 3) >> 2;
    };
#pragma clang loop unroll(disable)
    for (int tt = 2; tt < 16; ++tt) {
      int t = tt;
      asm volatile("" : "+s"(t));
      const int vc = vo + t * 65536, vn = t < 15 ? vc + 65536 : vo;
      const rsrc_t rn = rsrc((t < 15 ? p.kc : p.vc) + (size_t)bh * SK * DH, SK * DH * 2);
      level(t);
      float s[16];
      unroll<16>([&](auto I) {
        constexpr int j = I;
        s[j] = qk(r[j & 7]);
        SCHED();
        gate(md[j & 3]);
        if constexpr (j < 8) r[j & 7] = LD128(rk, vc, tile_off(j + 8), AUX_NT);
        else r[j & 7] = LD128(rn, vn, tile_off(j - 8), AUX_NT);
        SCHED();
      });
      const float sc = rbf(rbf(transpose_reduce(s, c)) * SCALE);
      sct[t * 64] = sc;
      mx = fmaxf(mx, sc);
    }
    {
      auto more = [&](auto I) {
        SCHED();
        VMWAIT(DEPTH - 1); k0[2 * I] = LD128(rv, vo, tile_off(8 + 2 * I), AUX_NT);
        VMWAIT(DEPTH - 1); k0[2 * I + 1] = LD128(rv, vo, tile_off(9 + 2 * I), AUX_NT);
        SCHED();
      };
      float e[16];
#pragma unroll
      for (int t = 0; t < 16; ++t) e[t] = sct[t * 64];
      more(ic<0>{});
      mx = wave_max(mx);
      float l0 = 0.f, l1 = 0.f;
#pragma unroll
      for (int t = 0; t < 16; t += 2) {
        e[t] = __builtin_amdgcn_exp2f((e[t] - mx) * LOG2E); e[t + 1] = __builtin_amdgcn_exp2f((e[t + 1] - mx) * LOG2E);
        l0 += e[t]; l1 += e[t + 1];
      }
      more(ic<1>{});
      const float ls = wave_sum(l0 + l1);
      LAS f32x2* ml = (LAS f32x2*)(L + L_PART);
      if (lane == 0) ml[w] = f32x2{mx, ls};
      arrive(C_SM);
      more(ic<2>{});
      more(ic<3>{});
      wait_lds(C_SM, 4u, ic<0>{});
      const f32x4 a0 = ((const LAS f32x4*)ml)[0], a1 = ((const LAS f32x4*)ml)[1];
      const float M = fmaxf(fmaxf(a0.x, a0.z), fmaxf(a1.x, a1.z));
      const float Ls = (a0.y * __builtin_amdgcn_exp2f((a0.x - M) * LOG2E) + a0.w * __builtin_amdgcn_exp2f((a0.z - M) * LOG2E)) +
                       (a1.y * __builtin_amdgcn_exp2f((a1.x - M) * LOG2E) + a1.w * __builtin_amdgcn_exp2f((a1.z - M) * LOG2E));
      const float f = __builtin_amdgcn_exp2f((mx - M) * LOG2E) / Ls;
#pragma unroll
      for (int t = 0; t < 16; ++t) sct[t * 64] = __uint_as_float(pk(e[t] * f, e[t] * f));
    }
    f32x4 acc = {0.f, 0.f, 0.f, 0.f};
    {
      const unsigned pp = __float_as_uint(sct[0]);
      level(16);
      unroll<8>([&](auto I) {
        acc = rowmac(r[I], dppu<BCAST + I>(pp), msk, acc);
        SCHED();
        VMWAIT(DEPTH - 1);
        r[I] = LD128(rv, vo, 65536 + tile_off(I), AUX_NT);
        SCHED();
      });
      unroll<8>([&](auto I) { acc = rowmac(k0[I], dppu<BCAST + 8 + I>(pp), msk, acc); });
      SCHED();
    }
#pragma clang loop unroll(disable)
    for (int tt = 1; tt < 16; ++tt) {
      int t = tt;
      asm volatile("" : "+s"(t));
      const int vc = vo + t * 65536, vn = vc + 65536;
      const unsigned pp = __float_as_uint(sct[t * 64]);
      if (t == 15) wait_lds(C_BOOT, 1u, ic<1>{});
      level(16 + t);
      unroll<16>([&](auto I) {
        constexpr int j = I;
        acc = rowmac(r[j & 7], dppu<BCAST + j>(pp), msk, acc);
        SCHED();
        gate(md[j & 3]);
        if constexpr (j < 8) r[j & 7] = LD128(rv, vc, tile_off(j + 8), AUX_NT);
        else r[j & 7] = LD128(rv, vn, tile_off(j - 8), AUX_NT);
        SCHED();
      });
    }
    LAS float* part = (LAS float*)(L + L_PART) + 8;
    if (c < 8) {
#pragma unroll
      for (int i = 0; i < 4; ++i) part[w * 128 + 32 * g + 8 * i + c] = acc[i];
    }
    asm volatile("s_waitcnt lgkmcnt(0)" ::: "memory");
    unsigned arr = 0;
    if (lane == 0) arr = __hip_atomic_fetch_add((LAS unsigned*)(L + L_CTL) + C_ARR, 1u, RLX_WG);
    if (__builtin_amdgcn_readfirstlane(arr) == 3u) {
      const LAS f32x2* pa = (const LAS f32x2*)part + lane;
      const f32x2 o2 = (pa[0] + pa[64]) + (pa[128] + pa[192]);
      __builtin_amdgcn_raw_buffer_store_b32(nosent(pk(o2.x, o2.y)), rws, WS_O + bh * 256 + lane * 4, 0, AUX_SC1);
      ctl[C_PROG] = 32u;
    }
    auto load_gu = [&](int i0, int i1, auto WIN) {
      const rsrc_t rw = rsrc(gm ? p.wu : p.wg, D * F * 2);
      const int wv = (gkh * 512 + r8) * (F * 2) + gcb * 128;
#pragma unroll 1
      for (int i = w * 16 + i0; i < w * 16 + i1; ++i) {
        VMWAIT(WIN - 1);
        dma(rw, lds0 + L_GU + i * 1024, wv + (nar_src ^ ((i & 1) << 6)), i * 8 * F * 2);
      }
    };
    if (ffn) {
      load_gu(0, GPRE, ic<8>{});
      wait_lds(C_OG, 4u, ic<1>{});
      load_gu(GPRE, 16, ic<LWIN>{});
      load_sq(p.wd, F, dhg * 128, dnb * 128, L_DN, ic<LWIN>{});
      VMWAIT(8);
      arrive(C_GU);
      DRAIN();
      arrive(C_DN);
    }
    return;
  }

  unsigned tag, tag_hi;
  LAS v4u* ax = (LAS v4u*)(L + L_XN);
  {
    float s1[2], s2[2];
#pragma unroll
    for (int rr = 0; rr < 2; ++rr) {
      float a0 = 0.f, a1 = 0.f, b0 = 0.f, b1 = 0.f;
#pragma unroll
      for (int i = 0; i < 4; ++i) {
        a0 = dot2(xr[2 * rr][i], 0x3F803F80u, a0); a1 = dot2(xr[2 * rr + 1][i], 0x3F803F80u, a1);
        b0 = dot2(xr[2 * rr][i], xr[2 * rr][i], b0); b1 = dot2(xr[2 * rr + 1][i], xr[2 * rr + 1][i], b1);
      }
      s1[rr] = wave_sum(a0 + a1); s2[rr] = wave_sum(b0 + b1);
    }
    const float mean = (lane < 32 ? s1[0] : s1[1]) * (1.f / D), var = fmaxf(fmaf(-mean, mean, (lane < 32 ? s2[0] : s2[1]) * (1.f / D)), 0.f);
    const float mb = rbf(mean), sd = rbf(__builtin_amdgcn_sqrtf(rbf(rbf(var) + 1e-5f))), rs = __builtin_amdgcn_rcpf(sd);
    v4u xn;
#pragma unroll
    for (int i = 0; i < 4; ++i) xn[i] = pk(divr(rbf(bflo(xs[i]) - mb), sd, rs), divr(rbf(bfhi(xs[i]) - mb), sd, rs));
    ax[(lane & 31) * 8 + 2 * hw + (lane >> 5)] = xn;
    const u64 tag64 = epoch * 0x9E3779B97F4A7C15ull + 0x632BE59BD9B4E019ull;
    tag = __builtin_amdgcn_readfirstlane((unsigned)tag64); tag_hi = __builtin_amdgcn_readfirstlane((unsigned)(tag64 >> 32));
    asm volatile("" ::"v"(code));
    DRAIN();
    arrive(C_XN);
    wait_lds(C_XN, 4u, ic<0>{});
    LAS unsigned char* wb = L + L_GU + (8 * g + q4) * 128 + ((hw ^ nar_f) << 5) + (lane & 3) * 8;
    f32x4 c0 = {0.f, 0.f, 0.f, 0.f}, c1 = c0;
#pragma unroll
    for (int s = 0; s < 8; s += 2) {
      c0 = mfma(trfrag(wb + s * 4096, 512), ax[(s * 4 + g) * 8 + rb], c0);
      c1 = mfma(trfrag(wb + s * 4096 + 4096, 512), ax[(s * 4 + 4 + g) * 8 + rb], c1);
    }
    c0 += c1;
    const int src = ((lane >> 1) & 3) * 16 + r8;
    const unsigned e0 = bperm(src, c0.x), e1 = bperm(src, c0.y), e2 = bperm(src, c0.z), e3 = bperm(src, c0.w);
    ST128((v4u{(lane & 1) ? e2 : e0, tag, (lane & 1) ? e3 : e1, tag_hi}), rws,
          WS_QX + (r8 * 32 + h) * 4096 + (b >> 1) * 1024 + (b & 1) * 512 + hw * 128 + (lane & 7) * 16);
    __builtin_amdgcn_s_sleep(QNAP);
    v4u e;
    for (unsigned it = 1;; ++it) {
      e = LD128(rws, WS_QX + bh * 4096 + hw * 1024 + lane * 16, 0, AUX_SC1);
      if (__builtin_amdgcn_ballot_w64(e.y != tag || e.w != tag_hi) == 0ull || TIMED_OUT(it)) break;
    }
    *(LAS v2u*)(L + L_QP + hw * 512 + lane * 8) = v2u{e.x, e.z};
  }
  BARRIER();
  if (hw == 3) {
    if (lane < 20) ST128((v4u{SENT, SENT, SENT, SENT}), rws, lane < 16 ? WS_O + bh * 256 + lane * 16 : WS_H + bh * 64 + (lane - 16) * 16);
    DRAIN();
    if (lane == 0) __hip_atomic_store((GAS u64*)(p.ws + WS_FLAGS) + bh, ((u64)tag_hi << 32) | tag, RLX_AGENT);
  } else if (hw == 0) {
    unsigned seen = 0, mine = 0, reported = 0, used = 0;
    for (unsigned it = 1; seen < 29u && !TIMED_OUT(it); ++it) {
      __builtin_amdgcn_s_sleep(8);
      const unsigned cur = __builtin_amdgcn_readfirstlane(ctl[C_PROG]);
      if (cur == seen) continue;
      seen = cur;
      const unsigned tn = now();
      if (cur / PER > reported) {
        reported = cur / PER;
        mine = tn;
        if (lane == 0 && ((xi + reported) & 7) == 0) __builtin_amdgcn_raw_buffer_store_b32(tn, rws, WS_BRD + reported * 1024 + ((xi >> 3) * 8 + xl) * 4, 0, AUX_SC1);
      } else if (reported > used) {
        used = reported;
        const unsigned x = __builtin_amdgcn_raw_buffer_load_b32(rws, WS_BRD + reported * 1024 + (lane & 31) * 4, 0, AUX_SC1);
        const float e = wave_sum((float)(int)(mine - (x - (t0 - 1000u) <= tn - t0 + 1000u ? x : tn))) * (0.01f / 64);
        const int lv = LV0 + (int)floorf(e * (e > 0.f ? KUP : KDN) + 0.5f);
        ctl[C_LEVEL] = lv < 0 ? 0 : lv > 16 ? 16 : lv;
      }
    }
    for (unsigned it = 1;; ++it) {
      const v4u f0 = LD128(rws, WS_FLAGS + lane * 16, 0, AUX_SC1), f1 = LD128(rws, WS_FLAGS + lane * 16, 1024, AUX_SC1);
      const bool ok = f0.x == tag && f0.y == tag_hi && f0.z == tag && f0.w == tag_hi && f1.x == tag && f1.y == tag_hi && f1.z == tag && f1.w == tag_hi;
      if (__builtin_amdgcn_ballot_w64(!ok) == 0ull || TIMED_OUT(it)) break;
      __builtin_amdgcn_s_sleep(16);
    }
    ctl[C_BOOT] = 1u;
  }
  auto sq_frags = [&](int lds, v4u (&wf)[8]) {
    LAS unsigned char* wb = L + lds + (8 * g + q4) * 256 + (lane & 3) * 8;
    const int f = q4 | ((g & 1) << 2);
#pragma unroll
    for (int s = 0; s < 4; ++s)
#pragma unroll
      for (int t = 0; t < 2; ++t) wf[2 * s + t] = trfrag(wb + s * 8192 + (((2 * hw + t) ^ f) << 5), 1024);
    SCHED();
  };
  auto sq_store = [&](f32x4 a0, f32x4 a1, int addr) {
    const v4u y = ror8(__builtin_bit_cast(v4u, a1));
    ST128(tagged(up ? y : __builtin_bit_cast(v4u, a0), tag), rws, addr);
  };
  const int sq_blk = 2 * (4 * hw + 2 * up + (g >> 1)) + (rb >> 2), sq_off = (rb & 3) * 32 + (g & 1) * 16;
  {
    int pa = WS_P1 + (32 * b + sq_blk) * 4096 + h * 128 + sq_off, oa = WS_O + (rb * 32 + h) * 256 + (32 * up + 8 * g) * 2;
    PIN(pa); PIN(oa);
    wait_lds(C_PROG, 30u, ic<16>{});
    wait_lds(C_PROG, 31u, ic<2>{});
    __builtin_amdgcn_s_sleep(WOAT);
    load_sq(p.wo, HD, h * 128, b * 128, L_WO, ic<WOWIN>{});
    DRAIN();
    arrive(C_WO);
    wait_lds(C_WO, 4u, ic<0>{});
    v4u wf[8], a0, a1;
    sq_frags(L_WO, wf);
    if (__builtin_amdgcn_readfirstlane(ctl[C_PROG]) < 32u) { wait_lds(C_PROG, 32u, ic<1>{}); __builtin_amdgcn_s_sleep(NAP1); }
    for (unsigned it = 1;; ++it) {
      a0 = LD128(rws, oa, 0, AUX_SC1); a1 = LD128(rws, oa, 128, AUX_SC1);
      if (__builtin_amdgcn_ballot_w64(max(umax4(a0), umax4(a1)) == SENT) == 0ull || TIMED_OUT(it)) break;
    }
    const v4u a0r = ror8(a0), a1r = ror8(a1);
    f32x4 c0 = {0.f, 0.f, 0.f, 0.f}, c1 = c0;
    c0 = mfma(wf[0], a0, c0); c1 = mfma(wf[1], a0, c1);
    c0 = mfma(wf[2], a0r, c0); c1 = mfma(wf[3], a0r, c1);
    c0 = mfma(wf[4], a1, c0); c1 = mfma(wf[5], a1, c1);
    c0 = mfma(wf[6], a1r, c0); c1 = mfma(wf[7], a1r, c1);
    sq_store(c0, c1, pa);
    SCHED();
    arrive(C_OG);
  }
  if (!ffn) {
    const int cb = ((xi - 28) * 8 + xl) * 4 + hw;
    const v2u xv = __builtin_amdgcn_raw_buffer_load_b64(rx, (c >> 1) * 2048 + cb * 16 + (c & 1) * 8, 0, 0);
    int ha = WS_H + (cb >> 3) * 1024 + (cb & 3) * 256 + ((cb >> 2) & 1) * 128 + c * 8;
    PIN(ha);
    v4u a[8];
    gather_tagged<8>(a, rws, WS_P1 + cb * 8192 + lane * 16, tag, t0);
    const f32x4 lo = (untag(a[0]) + untag(a[1])) + (untag(a[2]) + untag(a[3])), hi = (untag(a[4]) + untag(a[5])) + (untag(a[6]) + untag(a[7]));
    f32x4 s;
#pragma unroll
    for (int i = 0; i < 4; ++i) { const float l2 = lo[i] + dpp<XOR8>(lo[i]), h2 = hi[i] + dpp<XOR8>(hi[i]); s[i] = xrow_sum(up ? h2 : l2); }
    const v2u hv = {nosent(pk(bflo(xv.x) + rbf(s.x), bfhi(xv.x) + rbf(s.y))), nosent(pk(bflo(xv.y) + rbf(s.z), bfhi(xv.y) + rbf(s.w)))};
    if (lane < 16) __builtin_amdgcn_raw_buffer_store_b64(hv, rws, ha, 0, AUX_SC1);
    return;
  }
  const bool reducer = hw == 0 || (hw == 1 && n % 7 == 0);
  const int ri = hw == 0 ? n : 224 + n / 7;
  v2u hres;
  {
    int ga = WS_GP + (((gcb >> 1) * 2 + gm) * 2 + gkh) * 4096 + (2 * (gcb & 1) + (hw & 1)) * 1024 + lane * 16;
    int sa = L_STAT + (lane & 7) * 64, ca = L_RED + ((2 * (hw & 1) + (lane >> 5)) * 64 + ((lane >> 3) & 3) * 16 + 8) * 16, ra = ca + ((lane & 7) - 8) * 16;
    int hra = reducer ? WS_H + (ri >> 4) * 1024 + ((ri >> 1) & 3) * 256 + ((ri >> 3) & 1) * 128 + (ri & 1) * 64 + rb * 8 : WS_END;
    PIN(ga); PIN(sa); PIN(ca); PIN(ra); PIN(hra);
    wait_lds(C_GU, 4u, ic<0>{});
    v4u wf[16], a[4];
    LAS unsigned char* wb = L + L_GU + (128 * hw + 8 * g + q4) * 128 + (lane & 3) * 8;
    const int ho = WS_H + hw * 2048 + lane * 16;
    for (unsigned it = 1;; ++it) {
      a[0] = LD128(rws, ho, gkh * 8192, AUX_SC1); a[1] = LD128(rws, ho, gkh * 8192 + 1024, AUX_SC1);
      a[2] = LD128(rws, ho, 8192 - gkh * 8192, AUX_SC1); a[3] = LD128(rws, ho, 9216 - gkh * 8192, AUX_SC1);
      hres = __builtin_amdgcn_raw_buffer_load_b64(rws, hra, 0, AUX_SC1);
      if (it == 1u) {
        SCHED();
#pragma unroll
        for (int s = 0; s < 4; ++s)
#pragma unroll
          for (int t = 0; t < 4; ++t) wf[4 * s + t] = trfrag(wb + s * 4096 + ((t ^ nar_f) << 5), 512);
        SCHED();
      }
      if (__builtin_amdgcn_ballot_w64(max(max(max(umax4(a[0]), umax4(a[1])), max(umax4(a[2]), umax4(a[3]))), max(hres.x, hres.y)) == SENT) == 0ull || TIMED_OUT(it)) break;
    }
    const v4u ones = {0x3F803F80u, 0x3F803F80u, 0x3F803F80u, 0x3F803F80u};
    f32x4 acc[4];
#pragma unroll
    for (int t = 0; t < 4; ++t) acc[t] = f32x4{0.f, 0.f, 0.f, 0.f};
#pragma unroll
    for (int s = 0; s < 4; ++s) {
      const v4u src = (s & 1) ? ror8(a[s >> 1]) : a[s >> 1];
      const v4u xf = up ? ones : src;
#pragma unroll
      for (int t = 0; t < 4; ++t) acc[t] = mfma(wf[4 * s + t], xf, acc[t]);
    }
    f32x4 s1 = {0.f, 0.f, 0.f, 0.f}, s2 = s1;
#pragma unroll
    for (int u = 0; u < 4; ++u) { s1 = mfma(ones, a[u], s1); s2 = mfma(a[u], a[u], s2); }
    LAS f32x4* red = (LAS f32x4*)(L + L_RED);
#pragma unroll
    for (int t = 0; t < 4; ++t) red[(hw * 4 + t) * 64 + lane] = acc[t];
    LAS float* stat = (LAS float*)(L + L_STAT);
    if (c >> 2 == g) *(LAS f32x2*)(stat + rb * 16 + hw * 4 + up * 2) = f32x2{s1.x, (c & 2) ? ((c & 1) ? s2.w : s2.z) : ((c & 1) ? s2.y : s2.x)};
    BARRIER();
    if (hw < 2) {
      f32x4 rd[12];
#pragma unroll
      for (int i = 0; i < 4; ++i) { rd[i] = *(const LAS f32x4*)(L + sa + i * 16); rd[4 + i] = *(const LAS f32x4*)(L + ra + i * 4096); rd[8 + i] = *(const LAS f32x4*)(L + ca + i * 4096); }
      SCHED();
      const f32x4 st = (rd[0] + rd[1]) + (rd[2] + rd[3]), hs = (rd[4] + rd[5]) + (rd[6] + rd[7]), cs = (rd[8] + rd[9]) + (rd[10] + rd[11]);
      const float mean = (st.x + st.z) * (1.f / D), var = fmaxf(fmaf(-mean, mean, (st.y + st.w) * (1.f / D)), 0.f);
      const float mb = rbf(mean), rs = __builtin_amdgcn_rcpf(rbf(__builtin_amdgcn_sqrtf(rbf(rbf(var) + 1e-5f))));
      ST128(tagged(__builtin_bit_cast(v4u, (hs - mb * cs) * rs), tag), rws, ga);
    }
    if (tid == 448 && n == 0) __hip_atomic_store((GAS u64*)(p.ws + WS_EPOCH), epoch + 1ull, RLX_AGENT);
  }
  {
    int pa = WS_P2 + (32 * dnb + sq_blk) * 3584 + dhg * 128 + sq_off;
    PIN(pa);
    v4u wf[8], a[4];
    wait_lds(C_DN, 4u, ic<0>{});
    sq_frags(L_DN, wf);
    if (hw < 2) __builtin_amdgcn_s_sleep(NAP4 - 5); else __builtin_amdgcn_s_sleep(NAP4);
    gather_all<4>(a, rws, WS_GP + dhg * 16384 + hw * 1024 + lane * 16, 4096, tag, false, t0);
    const f32x4 gs = untag(a[0]) + untag(a[1]), us = untag(a[2]) + untag(a[3]);
    float o[4];
#pragma unroll
    for (int i = 0; i < 4; ++i) {
      const float x = rbf(gs[i]);
      const float y = 0.7978845608028654f * (x + 0.044715f * x * x * x);
      const float ge = x * __builtin_amdgcn_rcpf(1.f + __builtin_amdgcn_exp2f(-2.885390081777927f * y));
      o[i] = rbf(ge) * rbf(us[i]);
    }
    ((LAS v2u*)ax)[((hw * 4 + g) * 8 + rb) * 2 + up] = v2u{pk(o[0], o[1]), pk(o[2], o[3])};
    BARRIER();
    f32x4 c0 = {0.f, 0.f, 0.f, 0.f}, c1 = c0;
#pragma unroll
    for (int s = 0; s < 4; ++s) {
      const v4u xf = ax[(s * 4 + g) * 8 + rb];
      c0 = mfma(wf[2 * s], xf, c0); c1 = mfma(wf[2 * s + 1], xf, c1);
    }
    sq_store(c0, c1, pa);
  }
  if (reducer) {
    __builtin_amdgcn_s_sleep(NAP5);
    v4u a[4];
    gather_all<4>(a, rws, WS_P2 + ri * 3584 + lane * 16, 1024, tag, lane >= 32, t0);
    f32x4 s = (untag(a[0]) + untag(a[1])) + (untag(a[2]) + untag(a[3]));
#pragma unroll
    for (int i = 0; i < 4; ++i) s[i] = xrow_sum(s[i] + dpp<XOR8>(s[i]));
    const v2u ov = {pk(bflo(hres.x) + rbf(s.x), bfhi(hres.x) + rbf(s.y)), pk(bflo(hres.y) + rbf(s.z), bfhi(hres.y) + rbf(s.w))};
    if (lane < 8) *(GAS v2u*)(p.out + ((ri & 1) * 4 + (rb >> 1)) * D + (ri >> 1) * 8 + (rb & 1) * 4) = ov;
  }
}
}

extern "C" void kernel_launch(void* const* d_in, const int* in_sizes, int n_in, void* d_out, int out_size, void* d_ws, size_t ws_size,
                              hipStream_t stream) {
  Params p;
  p.x = (const u16*)d_in[0]; p.kc = (const u16*)d_in[1]; p.vc = (const u16*)d_in[2]; p.wq = (const u16*)d_in[3]; p.wo = (const u16*)d_in[4];
  p.wg = (const u16*)d_in[6]; p.wu = (const u16*)d_in[7]; p.wd = (const u16*)d_in[8];
  p.out = (u16*)d_out; p.ws = (unsigned char*)d_ws;
  hipLaunchKernelGGL(fused_decode_block, dim3(NWG), dim3(NTHR), 0, stream, p);
}
```

```cpp
#include <hip/hip_runtime.h>
#include <hip/hip_bf16.h>
#include <stdint.h>

namespace {
typedef unsigned short u16;
typedef unsigned long long u64;
typedef unsigned v4u __attribute__((ext_vector_type(4)));
typedef unsigned v2u __attribute__((ext_vector_type(2)));
typedef float f32x4 __attribute__((ext_vector_type(4)));
typedef float f32x2 __attribute__((ext_vector_type(2)));
typedef short bf16x8 __attribute__((ext_vector_type(8)));
typedef __bf16 bf16x2 __attribute__((ext_vector_type(2)));
typedef __amdgpu_buffer_rsrc_t rsrc_t;
#define GAS __attribute__((address_space(1)))
#define LAS __attribute__((address_space(3)))
#define RLX_AGENT __ATOMIC_RELAXED, __HIP_MEMORY_SCOPE_AGENT
#define RLX_WG __ATOMIC_RELAXED, __HIP_MEMORY_SCOPE_WORKGROUP
template <int N> using ic = std::integral_constant<int, N>;

constexpr int B = 8, D = 1024, H = 32, SK = 4096, DH = 128, F = 3584, HD = H * DH;
constexpr int NWG = 256, NTHR = 512, DEPTH = 6;
constexpr int PRE = 32;
constexpr int LV0 = 6;
constexpr unsigned PER = 3;
constexpr float KDN = 2.5f, KUP = 5.0f;
constexpr int WOAT = 36, WOWIN = 4;
constexpr int GPRE = 4, LWIN = 6;
constexpr int QNAP = 32, NAP1 = 8, NAP4 = 14, NAP5 = 14;
constexpr int AUX_NT = 2, AUX_SC1 = 16, RSRC3 = 0x00020000;
constexpr int WS_EPOCH = 0, WS_FLAGS = 2048;
constexpr int WS_O = 4096;
constexpr int WS_H = WS_O + B * HD * 2;
constexpr int WS_P1 = WS_H + B * D * 2;
constexpr int WS_GP = WS_P1 + 128 * H * 256;
constexpr int WS_P2 = WS_GP + 28 * 4 * 4096;
constexpr int WS_QX = WS_P2 + 128 * 28 * 256;
constexpr int WS_BRD = WS_QX + NWG * 4096;
constexpr int WS_END = WS_BRD + 16 * 1024;
constexpr int L_WO = 0, L_GU = 32768, L_DN = 98304, L_RED = 131072, L_XN = L_RED + 16384, L_QP = L_XN + 4096, L_PART = L_QP + 2048,
              L_STAT = L_PART + 2080, L_CTL = L_STAT + 512, L_END = L_CTL + 256;
enum { C_PROG, C_BOOT, C_ARR, C_XN, C_SM, C_WO, C_OG, C_GU, C_DN, C_LEVEL, C_RANK = 32 };
constexpr unsigned SENT = 0xFFFFFFFFu;
constexpr unsigned SPIN_TICKS = 2000000;

__device__ __forceinline__ float bflo(unsigned u) { return __builtin_bit_cast(float, u << 16); }
__device__ __forceinline__ float bfhi(unsigned u) { return __builtin_bit_cast(float, u & 0xffff0000u); }
__device__ __forceinline__ unsigned pk(float lo, float hi) {
  bf16x2 v = {(__bf16)lo, (__bf16)hi};
  return __builtin_bit_cast(unsigned, v);
}
__device__ __forceinline__ float rbf(float f) { return bflo(pk(f, f)); }
__device__ __forceinline__ float divr(float a, float d, float r) { const float q = a * r; return fmaf(fmaf(-q, d, a), r, q); }
__device__ __forceinline__ unsigned nosent(unsigned u) { return u == SENT ? 0x7FC07FC0u : u; }
__device__ __forceinline__ v4u tagged(v4u u, unsigned tag) {
  v4u r;
#pragma unroll
  for (int i = 0; i < 4; ++i) r[i] = ((u[i] + 0x80u) & 0xFFFFFF00u) | ((tag >> (8 * i)) & 0xFFu);
  return r;
}
__device__ __forceinline__ unsigned tagdiff(v4u u, unsigned tag) {
  return (__builtin_amdgcn_perm(u.y, u.x, 0x0c0c0400u) | __builtin_amdgcn_perm(u.w, u.z, 0x04000c0cu)) ^ tag;
}
__device__ __forceinline__ f32x4 untag(v4u u) { return __builtin_bit_cast(f32x4, u & 0xFFFFFF00u); }
template <int CT> __device__ __forceinline__ float dpp(float x) {
  return __builtin_bit_cast(float, __builtin_amdgcn_mov_dpp(__builtin_bit_cast(int, x), CT, 0xf, 0xf, true));
}
template <int CT> __device__ __forceinline__ unsigned dppu(unsigned x) { return (unsigned)__builtin_amdgcn_mov_dpp((int)x, CT, 0xf, 0xf, true); }
constexpr int XOR1 = 0xB1, XOR2 = 0x4E, XOR7 = 0x141, XOR8 = 0x128, BCAST = 0x150;
__device__ __forceinline__ float dot2(unsigned k, unsigned q, float acc) {
  return __builtin_amdgcn_fdot2_f32_bf16(__builtin_bit_cast(bf16x2, k), __builtin_bit_cast(bf16x2, q), acc, false);
}
__device__ __forceinline__ float xrow_max(float x) {
  auto s = __builtin_amdgcn_permlane16_swap(__float_as_uint(x), __float_as_uint(x), false, false);
  x = fmaxf(__uint_as_float(s[0]), __uint_as_float(s[1]));
  auto t = __builtin_amdgcn_permlane32_swap(__float_as_uint(x), __float_as_uint(x), false, false);
  return fmaxf(__uint_as_float(t[0]), __uint_as_float(t[1]));
}
__device__ __forceinline__ float xrow_sum(float x) {
  auto s = __builtin_amdgcn_permlane16_swap(__float_as_uint(x), __float_as_uint(x), false, false);
  x = __uint_as_float(s[0]) + __uint_as_float(s[1]);
  auto t = __builtin_amdgcn_permlane32_swap(__float_as_uint(x), __float_as_uint(x), false, false);
  return __uint_as_float(t[0]) + __uint_as_float(t[1]);
}
__device__ __forceinline__ float wave_max(float x) {
  x = fmaxf(x, dpp<XOR1>(x)); x = fmaxf(x, dpp<XOR2>(x)); x = fmaxf(x, dpp<XOR7>(x)); x = fmaxf(x, dpp<XOR8>(x));
  return xrow_max(x);
}
__device__ __forceinline__ float wave_sum(float x) {
  x += dpp<XOR1>(x); x += dpp<XOR2>(x); x += dpp<XOR7>(x); x += dpp<XOR8>(x);
  return xrow_sum(x);
}
__device__ __forceinline__ f32x4 mfma(v4u a, v4u b, f32x4 c) {
  return __builtin_amdgcn_mfma_f32_16x16x32_bf16(__builtin_bit_cast(bf16x8, a), __builtin_bit_cast(bf16x8, b), c, 0, 0, 0);
}
#define LD128(r, voff, soff, aux) __builtin_amdgcn_raw_buffer_load_b128((r), (voff), (soff), (aux))
#define ST128(v, r, voff) __builtin_amdgcn_raw_buffer_store_b128((v), (r), (voff), 0, AUX_SC1)
#define SCHED() __builtin_amdgcn_sched_barrier(0)
#define BARRIER() do { SCHED(); asm volatile("s_waitcnt lgkmcnt(0)\n\ts_barrier" ::: "memory"); SCHED(); } while (0)
#define DRAIN() asm volatile("s_waitcnt vmcnt(0)" ::: "memory")
#define VMWAIT(n) asm volatile("s_waitcnt vmcnt(%0)" ::"i"(n))
#define PIN(x) asm volatile("" : "+v"(x))
__device__ __forceinline__ unsigned now() { return (unsigned)__builtin_amdgcn_s_memrealtime(); }
__device__ __forceinline__ rsrc_t rsrc(const void* p, int bytes) { return __builtin_amdgcn_make_buffer_rsrc((void*)p, 0, bytes, RSRC3); }
__device__ __forceinline__ v4u ror8(v4u a) { return v4u{dppu<XOR8>(a.x), dppu<XOR8>(a.y), dppu<XOR8>(a.z), dppu<XOR8>(a.w)}; }
__device__ __forceinline__ unsigned umax4(v4u a) { return max(max(a.x, a.y), max(a.z, a.w)); }
__device__ __forceinline__ unsigned bperm(int src, float v) { return __builtin_amdgcn_ds_bpermute(src * 4, __float_as_uint(v)); }
__device__ __forceinline__ v4u trfrag(LAS unsigned char* p, int off2) {
  typedef short v4i16_t __attribute__((ext_vector_type(4)));
  v2u lo = __builtin_bit_cast(v2u, __builtin_amdgcn_ds_read_tr16_b64_v4i16((LAS v4i16_t*)p));
  v2u hi = __builtin_bit_cast(v2u, __builtin_amdgcn_ds_read_tr16_b64_v4i16((LAS v4i16_t*)(p + off2)));
  return v4u{lo.x, lo.y, hi.x, hi.y};
}
__device__ __forceinline__ f32x4 rowmac(v4u a, unsigned pb, v4u msk, f32x4 acc) { return mfma(a, v4u{pb & msk.x, pb & msk.y, pb & msk.z, pb & msk.w}, acc); }
template <int N, class Fn> __device__ __forceinline__ void unroll(Fn&& f) {
  if constexpr (N > 0) { unroll<N - 1>(f); f(ic<N - 1>{}); }
}
__device__ __forceinline__ float transpose_reduce(float (&x)[16], int c) {
#define TR_STEP(HALF, CT, BIT) _Pragma("unroll") for (int i = 0; i < (HALF); ++i) { \
    const float lo_ = x[i], hi_ = x[i + (HALF)]; const float t1_ = lo_ + dpp<CT>(lo_), t2_ = hi_ + dpp<CT>(hi_); x[i] = (c & (BIT)) ? t2_ : t1_; }
  TR_STEP(8, XOR8, 8) TR_STEP(4, XOR7, 4) TR_STEP(2, XOR2, 2) TR_STEP(1, XOR1, 1)
#undef TR_STEP
  return x[0];
}
constexpr int tile_off(int i) { return ((i / 4) * 16 + (i % 4)) * 1024; }
__device__ __forceinline__ void dma(rsrc_t rs, unsigned lds, int voff, int soff) {
  unsigned keep;
  asm volatile("s_mov_b32 %0, m0\n\ts_mov_b32 m0, %1\n\ts_nop 4\n\tbuffer_load_dwordx4 %2, %3, %4 offen lds\n\ts_mov_b32 m0, %0"
               : "=&s"(keep) : "s"(lds), "v"(voff), "s"(rs), "s"(soff) : "memory");
}
__device__ __forceinline__ void gate(int mode) {
  asm volatile("s_cmp_gt_u32 %0, 3\n\ts_cbranch_scc1 1f\n\ts_waitcnt vmcnt(6)\n\ts_cmp_gt_u32 %0, 2\n\ts_cbranch_scc1 1f\n\ts_waitcnt vmcnt(5)\n\t"
               "s_cmp_gt_u32 %0, 1\n\ts_cbranch_scc1 1f\n\ts_waitcnt vmcnt(4)\n\ts_cmp_gt_u32 %0, 0\n\ts_cbranch_scc1 1f\n\ts_waitcnt vmcnt(3)\n1:"
               ::"s"(mode) : "scc");
}
#define TIMED_OUT(it) (((it) & 63u) == 0u && now() - t0 > SPIN_TICKS)
template <int N> __device__ __forceinline__ void gather_tagged(v4u (&a)[N], rsrc_t rs, int voff, unsigned tag, unsigned t0) {
  unsigned todo = (1u << N) - 1u;
#pragma unroll
  for (int u = 0; u < N; ++u) a[u] = v4u{0u, 0u, 0u, 0u};
  for (unsigned it = 1;; ++it) {
#pragma unroll
    for (int u = 0; u < N; ++u) if (todo & (1u << u)) a[u] = LD128(rs, voff, u * 1024, AUX_SC1);
#pragma unroll
    for (int u = 0; u < N; ++u) if (__builtin_amdgcn_ballot_w64(tagdiff(a[u], tag) != 0u) == 0ull) todo &= ~(1u << u);
    if (todo == 0u || TIMED_OUT(it)) break;
  }
}

template <int N> __device__ __forceinline__ void gather_all(v4u (&a)[N], rsrc_t rs, int voff, int stride, unsigned tag, bool skip, unsigned t0) {
  for (unsigned it = 1;; ++it) {
#pragma unroll
    for (int u = 0; u < N; ++u) a[u] = LD128(rs, (u == N - 1 && skip) ? WS_END : voff, u * stride, AUX_SC1);
    unsigned m = skip ? 0u : tagdiff(a[N - 1], tag);
#pragma unroll
    for (int u = 0; u < N - 1; ++u) m |= tagdiff(a[u], tag);
    if (__builtin_amdgcn_ballot_w64(m != 0u) == 0ull || TIMED_OUT(it)) break;
  }
}

struct Params {
  const u16 *x, *kc, *vc, *wq, *wo, *wg, *wu, *wd;
  u16* out;
  unsigned char* ws;
};

__global__ void __launch_bounds__(NTHR, 2) fused_decode_block(Params p) {
  __shared__ __attribute__((aligned(16))) unsigned char smem[L_END];
  LAS unsigned char* L = (LAS unsigned char*)smem;
  volatile LAS unsigned* ctl = (volatile LAS unsigned*)(L + L_CTL);
  const unsigned lds0 = (unsigned)(size_t)L;
  const int tid = threadIdx.x, lane = tid & 63, w = __builtin_amdgcn_readfirstlane(tid >> 6), hw = w & 3;
  const int g = lane >> 4, c = lane & 15, rb = c & 7, up = c >> 3;
  const int bh = blockIdx.x, b = bh >> 5, h = bh & 31;
  const unsigned t0 = now();
  const rsrc_t rws = rsrc(p.ws, WS_END);
  auto wait_lds = [&](int word, unsigned want, auto NAP) {
    for (unsigned it = 1; ctl[word] < want; ++it) {
      if (NAP) __builtin_amdgcn_s_sleep(decltype(NAP)::value);
      if (TIMED_OUT(it)) break;
    }
    asm volatile("" ::: "memory");
  };
  auto arrive = [&](int word) {
    asm volatile("s_waitcnt lgkmcnt(0)" ::: "memory");
    if (lane == 0) __hip_atomic_fetch_add((LAS unsigned*)(L + L_CTL) + word, 1u, RLX_WG);
  };
  const int r8 = lane >> 3, nar_src = (((lane >> 1) & 3) ^ ((r8 >> 1) & 1)) << 5 | (lane & 1) << 4;
  const int q4 = (lane >> 2) & 3, nar_f = (q4 >> 1) | ((g & 1) << 1);

  const rsrc_t rk = rsrc(p.kc + (size_t)bh * SK * DH, SK * DH * 2), rx = rsrc(p.x, B * D * 2);
  const int vo = hw * 4096 + lane * 16;
  v4u k0[16], xr[4], xs;
  u64 epoch;
  if (w < 4) {
#pragma unroll
    for (int i = 0; i < DEPTH; ++i) k0[i] = LD128(rk, vo, tile_off(i), AUX_NT);
    SCHED();
  } else {
#pragma unroll
    for (int i = 0; i < 4; ++i) xr[i] = LD128(rx, vo, i * 1024, 0);
    xs = LD128(rx, (2 * hw + (lane >> 5)) * 2048 + (b >> 1) * 512 + (lane & 31) * 16, 0, 0);
    epoch = __hip_atomic_load((GAS u64*)(p.ws + WS_EPOCH), RLX_AGENT);
    SCHED();
    const rsrc_t rw = rsrc(p.wq, D * HD * 2);
    const int qv = ((b >> 1) * 256 + hw * 64 + r8) * (HD * 2) + h * 256 + (b & 1) * 128;
#pragma unroll
    for (int i = 0; i < 8; ++i)
      __builtin_amdgcn_raw_ptr_buffer_load_lds(rw, (LAS void*)(L + L_GU + (hw * 8 + i) * 1024), 16, qv + (nar_src ^ ((i & 1) << 6)), i * 8 * HD * 2, 0, 0);
    if (w == 4) ctl[lane] = lane == C_LEVEL ? LV0 : 0u;
  }
  BARRIER();

  constexpr float SCALE = 0.08838834764831845f, LOG2E = 1.4426950408889634f;
  const int xl = bh & 7, xi = bh >> 3, n = xl * 28 + xi;
  const bool ffn = xi < 28;
  const int gm = n / 112, gcb = (n >> 1) % 56, gkh = n & 1;
  const int dhg = n >> 3, dnb = n & 7;
  auto load_sq = [&](const u16* base, int rows, int row0, int col0, int lds, auto WIN) {
    const rsrc_t rw = rsrc(base, rows * D * 2);
    const int wv = (row0 + g) * (D * 2) + col0 * 2 + (c & 1) * 16;
#pragma unroll 1
    for (int i = hw * 8; i < hw * 8 + 8; ++i) {
      VMWAIT(WIN - 1);
      dma(rw, lds0 + lds + i * 1024, wv + (((c >> 1) ^ g ^ ((i << 1) & 4)) << 5), i * 4 * D * 2);
    }
  };

  if (w < 4) {
    const rsrc_t rv = rsrc(p.vc + (size_t)bh * SK * DH, SK * DH * 2);
    const unsigned half = up ? 0u : (c & 1) ? 0xFFFF0000u : 0x0000FFFFu;
    const v4u msk = {rb >> 1 == 0 ? half : 0u, rb >> 1 == 1 ? half : 0u, rb >> 1 == 2 ? half : 0u, rb >> 1 == 3 ? half : 0u};
    v4u k1[16], r[8];
    auto first = [&](auto I) {
      constexpr int i = I;
      VMWAIT(DEPTH - 1);
      if constexpr (i < 16) k0[i] = LD128(rk, vo, tile_off(i), AUX_NT);
      else if constexpr (i < 32) k1[i - 16] = LD128(rk, vo, 65536 + tile_off(i - 16), AUX_NT);
      else r[i - 32] = LD128(rk, vo, 131072 + tile_off(i - 32), AUX_NT);
      SCHED();
    };
    unroll<PRE - DEPTH>([&](auto J) { first(ic<DEPTH + J>{}); });
    BARRIER();
    auto rest = [&](auto STEP) {
      constexpr int lo = PRE + (40 - PRE) * STEP / 8, hi = PRE + (40 - PRE) * (STEP + 1) / 8;
      SCHED();
      unroll<hi - lo>([&](auto I) { first(ic<lo + I>{}); });
    };
    static_assert(PRE >= 32, "tiles 0 and 1 are requested before q is waited for");
    unsigned q[4];
    {
      const LAS f32x4* qp = (const LAS f32x4*)(L + L_QP);
      const f32x4 lo = (qp[2 * c] + qp[32 + 2 * c]) + (qp[64 + 2 * c] + qp[96 + 2 * c]);
      const f32x4 hi = (qp[2 * c + 1] + qp[33 + 2 * c]) + (qp[65 + 2 * c] + qp[97 + 2 * c]);
      q[0] = pk(lo.x, lo.y); q[1] = pk(lo.z, lo.w); q[2] = pk(hi.x, hi.y); q[3] = pk(hi.z, hi.w);
    }
    LAS float* sct = (LAS float*)(L + L_RED) + w * 1024 + lane;
    auto qk = [&](v4u x) { return dot2(x.w, q[3], dot2(x.z, q[2], dot2(x.y, q[1], dot2(x.x, q[0], 0.f)))); };
    float mx;
    {
      float s[16];
      unroll<16>([&](auto I) { if constexpr (I % 4 == 0) rest(ic<I / 4>{}); s[I] = qk(k0[I]); });
      mx = rbf(rbf(transpose_reduce(s, c)) * SCALE);
      sct[0] = mx;
      unroll<16>([&](auto I) { if constexpr (I % 4 == 0) rest(ic<4 + I / 4>{}); s[I] = qk(k1[I]); });
      const float sc = rbf(rbf(transpose_reduce(s, c)) * SCALE);
      sct[64] = sc;
      mx = fmaxf(mx, sc);
    }
    SCHED();
    int md[4];
    auto level = [&](int t) {
      unsigned rank = 0;
      if (lane == 0) rank = __hip_atomic_fetch_add((LAS unsigned*)(L + L_CTL) + C_RANK + t, 1u, RLX_WG);
      int lv = ctl[C_LEVEL];
      rank = __builtin_amdgcn_readfirstlane(rank);
      if (rank == 1u) ctl[C_PROG] = t;
      lv = __builtin_amdgcn_readfirstlane(lv) + (rank == 3u) - (rank == 0u);
      lv = __builtin_amdgcn_readfirstlane(lv < 0 ? 0 : lv > 16 ? 16 : lv);
      md[0] = lv >> 2; md[1] = (lv + 2) >> 2; md[2] = (lv + 1) >> 2; md[3] = (lv + 3) >> 2;
    };
#pragma clang loop unroll(disable)
    for (int tt = 2; tt < 16; ++tt) {
      int t = tt;
      asm volatile("" : "+s"(t));
      const int vc = vo + t * 65536, vn = t < 15 ? vc + 65536 : vo;
      const rsrc_t rn = rsrc((t < 15 ? p.kc : p.vc) + (size_t)bh * SK * DH, SK * DH * 2);
      level(t);
      float s[16];
      unroll<16>([&](auto I) {
        constexpr int j = I;
        s[j] = qk(r[j & 7]);
        SCHED();
        gate(md[j & 3]);
        if constexpr (j < 8) r[j & 7] = LD128(rk, vc, tile_off(j + 8), AUX_NT);
        else r[j & 7] = LD128(rn, vn, tile_off(j - 8), AUX_NT);
        SCHED();
      });
      const float sc = rbf(rbf(transpose_reduce(s, c)) * SCALE);
      sct[t * 64] = sc;
      mx = fmaxf(mx, sc);
    }
    {
      auto more = [&](auto I) {
        SCHED();
        VMWAIT(DEPTH - 1); k0[2 * I] = LD128(rv, vo, tile_off(8 + 2 * I), AUX_NT);
        VMWAIT(DEPTH - 1); k0[2 * I + 1] = LD128(rv, vo, tile_off(9 + 2 * I), AUX_NT);
        SCHED();
      };
      float e[16];
#pragma unroll
      for (int t = 0; t < 16; ++t) e[t] = sct[t * 64];
      more(ic<0>{});
      mx = wave_max(mx);
      float l0 = 0.f, l1 = 0.f;
#pragma unroll
      for (int t = 0; t < 16; t += 2) {
        e[t] = __builtin_amdgcn_exp2f((e[t] - mx) * LOG2E); e[t + 1] = __builtin_amdgcn_exp2f((e[t + 1] - mx) * LOG2E);
        l0 += e[t]; l1 += e[t + 1];
      }
      more(ic<1>{});
      const float ls = wave_sum(l0 + l1);
      LAS f32x2* ml = (LAS f32x2*)(L + L_PART);
      if (lane == 0) ml[w] = f32x2{mx, ls};
      arrive(C_SM);
      more(ic<2>{});
      more(ic<3>{});
      wait_lds(C_SM, 4u, ic<0>{});
      const f32x4 a0 = ((const LAS f32x4*)ml)[0], a1 = ((const LAS f32x4*)ml)[1];
      const float M = fmaxf(fmaxf(a0.x, a0.z), fmaxf(a1.x, a1.z));
      const float Ls = (a0.y * __builtin_amdgcn_exp2f((a0.x - M) * LOG2E) + a0.w * __builtin_amdgcn_exp2f((a0.z - M) * LOG2E)) +
                       (a1.y * __builtin_amdgcn_exp2f((a1.x - M) * LOG2E) + a1.w * __builtin_amdgcn_exp2f((a1.z - M) * LOG2E));
      const float f = __builtin_amdgcn_exp2f((mx - M) * LOG2E) / Ls;
#pragma unroll
      for (int t = 0; t < 16; ++t) sct[t * 64] = __uint_as_float(pk(e[t] * f, e[t] * f));
    }
    f32x4 acc = {0.f, 0.f, 0.f, 0.f};
    {
      const unsigned pp = __float_as_uint(sct[0]);
      level(16);
      unroll<8>([&](auto I) {
        acc = rowmac(r[I], dppu<BCAST + I>(pp), msk, acc);
        SCHED();
        VMWAIT(DEPTH - 1);
        r[I] = LD128(rv, vo, 65536 + tile_off(I), AUX_NT);
        SCHED();
      });
      unroll<8>([&](auto I) { acc = rowmac(k0[I], dppu<BCAST + 8 + I>(pp), msk, acc); });
      SCHED();
    }
#pragma clang loop unroll(disable)
    for (int tt = 1; tt < 16; ++tt) {
      int t = tt;
      asm volatile("" : "+s"(t));
      const int vc = vo + t * 65536, vn = vc + 65536;
      const unsigned pp = __float_as_uint(sct[t * 64]);
      if (t == 15) wait_lds(C_BOOT, 1u, ic<1>{});
      level(16 + t);
      unroll<16>([&](auto I) {
        constexpr int j = I;
        acc = rowmac(r[j & 7], dppu<BCAST + j>(pp), msk, acc);
        SCHED();
        gate(md[j & 3]);
        if constexpr (j < 8) r[j & 7] = LD128(rv, vc, tile_off(j + 8), AUX_NT);
        else r[j & 7] = LD128(rv, vn, tile_off(j - 8), AUX_NT);
        SCHED();
      });
    }
    LAS float* part = (LAS float*)(L + L_PART) + 8;
    if (c < 8) {
#pragma unroll
      for (int i = 0; i < 4; ++i) part[w * 128 + 32 * g + 8 * i + c] = acc[i];
    }
    asm volatile("s_waitcnt lgkmcnt(0)" ::: "memory");
    unsigned arr = 0;
    if (lane == 0) arr = __hip_atomic_fetch_add((LAS unsigned*)(L + L_CTL) + C_ARR, 1u, RLX_WG);
    if (__builtin_amdgcn_readfirstlane(arr) == 3u) {
      const LAS f32x2* pa = (const LAS f32x2*)part + lane;
      const f32x2 o2 = (pa[0] + pa[64]) + (pa[128] + pa[192]);
      __builtin_amdgcn_raw_buffer_store_b32(nosent(pk(o2.x, o2.y)), rws, WS_O + bh * 256 + lane * 4, 0, AUX_SC1);
      ctl[C_PROG] = 32u;
    }
    auto load_gu = [&](int i0, int i1, auto WIN) {
      const rsrc_t rw = rsrc(gm ? p.wu : p.wg, D * F * 2);
      const int wv = (gkh * 512 + r8) * (F * 2) + gcb * 128;
#pragma unroll 1
      for (int i = w * 16 + i0; i < w * 16 + i1; ++i) {
        VMWAIT(WIN - 1);
        dma(rw, lds0 + L_GU + i * 1024, wv + (nar_src ^ ((i & 1) << 6)), i * 8 * F * 2);
      }
    };
    if (ffn) {
      load_gu(0, GPRE, ic<8>{});
      wait_lds(C_OG, 4u, ic<1>{});
      load_gu(GPRE, 16, ic<LWIN>{});
      load_sq(p.wd, F, dhg * 128, dnb * 128, L_DN, ic<LWIN>{});
      VMWAIT(8);
      arrive(C_GU);
      DRAIN();
      arrive(C_DN);
    }
    return;
  }

  unsigned tag, tag_hi;
  LAS v4u* ax = (LAS v4u*)(L + L_XN);
  {
    float s1[2], s2[2];
#pragma unroll
    for (int rr = 0; rr < 2; ++rr) {
      float a0 = 0.f, a1 = 0.f, b0 = 0.f, b1 = 0.f;
#pragma unroll
      for (int i = 0; i < 4; ++i) {
        a0 = dot2(xr[2 * rr][i], 0x3F803F80u, a0); a1 = dot2(xr[2 * rr + 1][i], 0x3F803F80u, a1);
        b0 = dot2(xr[2 * rr][i], xr[2 * rr][i], b0); b1 = dot2(xr[2 * rr + 1][i], xr[2 * rr + 1][i], b1);
      }
      s1[rr] = wave_sum(a0 + a1); s2[rr] = wave_sum(b0 + b1);
    }
    const float mean = (lane < 32 ? s1[0] : s1[1]) * (1.f / D), var = fmaxf(fmaf(-mean, mean, (lane < 32 ? s2[0] : s2[1]) * (1.f / D)), 0.f);
    const float mb = rbf(mean), sd = rbf(__builtin_amdgcn_sqrtf(rbf(rbf(var) + 1e-5f))), rs = __builtin_amdgcn_rcpf(sd);
    v4u xn;
#pragma unroll
    for (int i = 0; i < 4; ++i) xn[i] = pk(divr(rbf(bflo(xs[i]) - mb), sd, rs), divr(rbf(bfhi(xs[i]) - mb), sd, rs));
    ax[(lane & 31) * 8 + 2 * hw + (lane >> 5)] = xn;
    const u64 tag64 = epoch * 0x9E3779B97F4A7C15ull + 0x632BE59BD9B4E019ull;
    tag = __builtin_amdgcn_readfirstlane((unsigned)tag64); tag_hi = __builtin_amdgcn_readfirstlane((unsigned)(tag64 >> 32));
    DRAIN();
    arrive(C_XN);
    wait_lds(C_XN, 4u, ic<0>{});
    LAS unsigned char* wb = L + L_GU + (8 * g + q4) * 128 + ((hw ^ nar_f) << 5) + (lane & 3) * 8;
    f32x4 c0 = {0.f, 0.f, 0.f, 0.f}, c1 = c0;
#pragma unroll
    for (int s = 0; s < 8; s += 2) {
      c0 = mfma(trfrag(wb + s * 4096, 512), ax[(s * 4 + g) * 8 + rb], c0);
      c1 = mfma(trfrag(wb + s * 4096 + 4096, 512), ax[(s * 4 + 4 + g) * 8 + rb], c1);
    }
    c0 += c1;
    const int src = ((lane >> 1) & 3) * 16 + r8;
    const unsigned e0 = bperm(src, c0.x), e1 = bperm(src, c0.y), e2 = bperm(src, c0.z), e3 = bperm(src, c0.w);
    ST128((v4u{(lane & 1) ? e2 : e0, tag, (lane & 1) ? e3 : e1, tag_hi}), rws,
          WS_QX + (r8 * 32 + h) * 4096 + (b >> 1) * 1024 + (b & 1) * 512 + hw * 128 + (lane & 7) * 16);
    __builtin_amdgcn_s_sleep(QNAP);
    v4u e;
    for (unsigned it = 1;; ++it) {
      e = LD128(rws, WS_QX + bh * 4096 + hw * 1024 + lane * 16, 0, AUX_SC1);
      if (__builtin_amdgcn_ballot_w64(e.y != tag || e.w != tag_hi) == 0ull || TIMED_OUT(it)) break;
    }
    *(LAS v2u*)(L + L_QP + hw * 512 + lane * 8) = v2u{e.x, e.z};
  }
  BARRIER();
  if (hw == 3) {
    if (lane < 20) ST128((v4u{SENT, SENT, SENT, SENT}), rws, lane < 16 ? WS_O + bh * 256 + lane * 16 : WS_H + bh * 64 + (lane - 16) * 16);
    DRAIN();
    if (lane == 0) __hip_atomic_store((GAS u64*)(p.ws + WS_FLAGS) + bh, ((u64)tag_hi << 32) | tag, RLX_AGENT);
  } else if (hw == 0) {
    unsigned seen = 0, mine = 0, reported = 0, used = 0;
    for (unsigned it = 1; seen < 29u && !TIMED_OUT(it); ++it) {
      __builtin_amdgcn_s_sleep(8);
      const unsigned cur = __builtin_amdgcn_readfirstlane(ctl[C_PROG]);
      if (cur == seen) continue;
      seen = cur;
      const unsigned tn = now();
      if (cur / PER > reported) {
        reported = cur / PER;
        mine = tn;
        if (lane == 0 && ((xi + reported) & 7) == 0) __builtin_amdgcn_raw_buffer_store_b32(tn, rws, WS_BRD + reported * 1024 + ((xi >> 3) * 8 + xl) * 4, 0, AUX_SC1);
      } else if (reported > used) {
        used = reported;
        const unsigned x = __builtin_amdgcn_raw_buffer_load_b32(rws, WS_BRD + reported * 1024 + (lane & 31) * 4, 0, AUX_SC1);
        const float e = wave_sum((float)(int)(mine - (x - (t0 - 1000u) <= tn - t0 + 1000u ? x : tn))) * (0.01f / 64);
        const int lv = LV0 + (int)floorf(e * (e > 0.f ? KUP : KDN) + 0.5f);
        ctl[C_LEVEL] = lv < 0 ? 0 : lv > 16 ? 16 : lv;
      }
    }
    for (unsigned it = 1;; ++it) {
      const v4u f0 = LD128(rws, WS_FLAGS + lane * 16, 0, AUX_SC1), f1 = LD128(rws, WS_FLAGS + lane * 16, 1024, AUX_SC1);
      const bool ok = f0.x == tag && f0.y == tag_hi && f0.z == tag && f0.w == tag_hi && f1.x == tag && f1.y == tag_hi && f1.z == tag && f1.w == tag_hi;
      if (__builtin_amdgcn_ballot_w64(!ok) == 0ull || TIMED_OUT(it)) break;
      __builtin_amdgcn_s_sleep(16);
    }
    ctl[C_BOOT] = 1u;
  }
  auto sq_frags = [&](int lds, v4u (&wf)[8]) {
    LAS unsigned char* wb = L + lds + (8 * g + q4) * 256 + (lane & 3) * 8;
    const int f = q4 | ((g & 1) << 2);
#pragma unroll
    for (int s = 0; s < 4; ++s)
#pragma unroll
      for (int t = 0; t < 2; ++t) wf[2 * s + t] = trfrag(wb + s * 8192 + (((2 * hw + t) ^ f) << 5), 1024);
    SCHED();
  };
  auto sq_store = [&](f32x4 a0, f32x4 a1, int addr) {
    const v4u y = ror8(__builtin_bit_cast(v4u, a1));
    ST128(tagged(up ? y : __builtin_bit_cast(v4u, a0), tag), rws, addr);
  };
  const int sq_blk = 2 * (4 * hw + 2 * up + (g >> 1)) + (rb >> 2), sq_off = (rb & 3) * 32 + (g & 1) * 16;
  {
    int pa = WS_P1 + (32 * b + sq_blk) * 4096 + h * 128 + sq_off, oa = WS_O + (rb * 32 + h) * 256 + (32 * up + 8 * g) * 2;
    PIN(pa); PIN(oa);
    wait_lds(C_PROG, 30u, ic<16>{});
    wait_lds(C_PROG, 31u, ic<2>{});
    __builtin_amdgcn_s_sleep(WOAT);
    load_sq(p.wo, HD, h * 128, b * 128, L_WO, ic<WOWIN>{});
    DRAIN();
    arrive(C_WO);
    wait_lds(C_WO, 4u, ic<0>{});
    v4u wf[8], a0, a1;
    sq_frags(L_WO, wf);
    if (__builtin_amdgcn_readfirstlane(ctl[C_PROG]) < 32u) { wait_lds(C_PROG, 32u, ic<1>{}); __builtin_amdgcn_s_sleep(NAP1); }
    for (unsigned it = 1;; ++it) {
      a0 = LD128(rws, oa, 0, AUX_SC1); a1 = LD128(rws, oa, 128, AUX_SC1);
      if (__builtin_amdgcn_ballot_w64(max(umax4(a0), umax4(a1)) == SENT) == 0ull || TIMED_OUT(it)) break;
    }
    const v4u a0r = ror8(a0), a1r = ror8(a1);
    f32x4 c0 = {0.f, 0.f, 0.f, 0.f}, c1 = c0;
    c0 = mfma(wf[0], a0, c0); c1 = mfma(wf[1], a0, c1);
    c0 = mfma(wf[2], a0r, c0); c1 = mfma(wf[3], a0r, c1);
    c0 = mfma(wf[4], a1, c0); c1 = mfma(wf[5], a1, c1);
    c0 = mfma(wf[6], a1r, c0); c1 = mfma(wf[7], a1r, c1);
    sq_store(c0, c1, pa);
    SCHED();
    arrive(C_OG);
  }
  if (!ffn) {
    const int cb = ((xi - 28) * 8 + xl) * 4 + hw;
    const v2u xv = __builtin_amdgcn_raw_buffer_load_b64(rx, (c >> 1) * 2048 + cb * 16 + (c & 1) * 8, 0, 0);
    int ha = WS_H + (cb >> 3) * 1024 + (cb & 3) * 256 + ((cb >> 2) & 1) * 128 + c * 8;
    PIN(ha);
    v4u a[8];
    gather_tagged<8>(a, rws, WS_P1 + cb * 8192 + lane * 16, tag, t0);
    const f32x4 lo = (untag(a[0]) + untag(a[1])) + (untag(a[2]) + untag(a[3])), hi = (untag(a[4]) + untag(a[5])) + (untag(a[6]) + untag(a[7]));
    f32x4 s;
#pragma unroll
    for (int i = 0; i < 4; ++i) { const float l2 = lo[i] + dpp<XOR8>(lo[i]), h2 = hi[i] + dpp<XOR8>(hi[i]); s[i] = xrow_sum(up ? h2 : l2); }
    const v2u hv = {nosent(pk(bflo(xv.x) + rbf(s.x), bfhi(xv.x) + rbf(s.y))), nosent(pk(bflo(xv.y) + rbf(s.z), bfhi(xv.y) + rbf(s.w)))};
    if (lane < 16) __builtin_amdgcn_raw_buffer_store_b64(hv, rws, ha, 0, AUX_SC1);
    return;
  }
  const bool reducer = hw == 0 || (hw == 1 && n % 7 == 0);
  const int ri = hw == 0 ? n : 224 + n / 7;
  v2u hres;
  {
    int ga = WS_GP + (((gcb >> 1) * 2 + gm) * 2 + gkh) * 4096 + (2 * (gcb & 1) + (hw & 1)) * 1024 + lane * 16;
    int sa = L_STAT + (lane & 7) * 64, ca = L_RED + ((2 * (hw & 1) + (lane >> 5)) * 64 + ((lane >> 3) & 3) * 16 + 8) * 16, ra = ca + ((lane & 7) - 8) * 16;
    int hra = reducer ? WS_H + (ri >> 4) * 1024 + ((ri >> 1) & 3) * 256 + ((ri >> 3) & 1) * 128 + (ri & 1) * 64 + rb * 8 : WS_END;
    PIN(ga); PIN(sa); PIN(ca); PIN(ra); PIN(hra);
    wait_lds(C_GU, 4u, ic<0>{});
    v4u wf[16], a[4];
    LAS unsigned char* wb = L + L_GU + (128 * hw + 8 * g + q4) * 128 + (lane & 3) * 8;
    const int ho = WS_H + hw * 2048 + lane * 16;
    for (unsigned it = 1;; ++it) {
      a[0] = LD128(rws, ho, gkh * 8192, AUX_SC1); a[1] = LD128(rws, ho, gkh * 8192 + 1024, AUX_SC1);
      a[2] = LD128(rws, ho, 8192 - gkh * 8192, AUX_SC1); a[3] = LD128(rws, ho, 9216 - gkh * 8192, AUX_SC1);
      hres = __builtin_amdgcn_raw_buffer_load_b64(rws, hra, 0, AUX_SC1);
      if (it == 1u) {
        SCHED();
#pragma unroll
        for (int s = 0; s < 4; ++s)
#pragma unroll
          for (int t = 0; t < 4; ++t) wf[4 * s + t] = trfrag(wb + s * 4096 + ((t ^ nar_f) << 5), 512);
        SCHED();
      }
      if (__builtin_amdgcn_ballot_w64(max(max(max(umax4(a[0]), umax4(a[1])), max(umax4(a[2]), umax4(a[3]))), max(hres.x, hres.y)) == SENT) == 0ull || TIMED_OUT(it)) break;
    }
    const v4u ones = {0x3F803F80u, 0x3F803F80u, 0x3F803F80u, 0x3F803F80u};
    f32x4 acc[4];
#pragma unroll
    for (int t = 0; t < 4; ++t) acc[t] = f32x4{0.f, 0.f, 0.f, 0.f};
#pragma unroll
    for (int s = 0; s < 4; ++s) {
      const v4u src = (s & 1) ? ror8(a[s >> 1]) : a[s >> 1];
      const v4u xf = up ? ones : src;
#pragma unroll
      for (int t = 0; t < 4; ++t) acc[t] = mfma(wf[4 * s + t], xf, acc[t]);
    }
    f32x4 s1 = {0.f, 0.f, 0.f, 0.f}, s2 = s1;
#pragma unroll
    for (int u = 0; u < 4; ++u) { s1 = mfma(ones, a[u], s1); s2 = mfma(a[u], a[u], s2); }
    LAS f32x4* red = (LAS f32x4*)(L + L_RED);
#pragma unroll
    for (int t = 0; t < 4; ++t) red[(hw * 4 + t) * 64 + lane] = acc[t];
    LAS float* stat = (LAS float*)(L + L_STAT);
    if (c >> 2 == g) *(LAS f32x2*)(stat + rb * 16 + hw * 4 + up * 2) = f32x2{s1.x, (c & 2) ? ((c & 1) ? s2.w : s2.z) : ((c & 1) ? s2.y : s2.x)};
    BARRIER();
    if (hw < 2) {
      f32x4 rd[12];
#pragma unroll
      for (int i = 0; i < 4; ++i) { rd[i] = *(const LAS f32x4*)(L + sa + i * 16); rd[4 + i] = *(const LAS f32x4*)(L + ra + i * 4096); rd[8 + i] = *(const LAS f32x4*)(L + ca + i * 4096); }
      SCHED();
      const f32x4 st = (rd[0] + rd[1]) + (rd[2] + rd[3]), hs = (rd[4] + rd[5]) + (rd[6] + rd[7]), cs = (rd[8] + rd[9]) + (rd[10] + rd[11]);
      const float mean = (st.x + st.z) * (1.f / D), var = fmaxf(fmaf(-mean, mean, (st.y + st.w) * (1.f / D)), 0.f);
      const float mb = rbf(mean), rs = __builtin_amdgcn_rcpf(rbf(__builtin_amdgcn_sqrtf(rbf(rbf(var) + 1e-5f))));
      ST128(tagged(__builtin_bit_cast(v4u, (hs - mb * cs) * rs), tag), rws, ga);
    }
    if (tid == 448 && n == 0) __hip_atomic_store((GAS u64*)(p.ws + WS_EPOCH), epoch + 1ull, RLX_AGENT);
  }
  {
    int pa = WS_P2 + (32 * dnb + sq_blk) * 3584 + dhg * 128 + sq_off;
    PIN(pa);
    v4u wf[8], a[4];
    wait_lds(C_DN, 4u, ic<0>{});
    sq_frags(L_DN, wf);
    if (hw < 2) __builtin_amdgcn_s_sleep(NAP4 - 5); else __builtin_amdgcn_s_sleep(NAP4);
    gather_all<4>(a, rws, WS_GP + dhg * 16384 + hw * 1024 + lane * 16, 4096, tag, false, t0);
    const f32x4 gs = untag(a[0]) + untag(a[1]), us = untag(a[2]) + untag(a[3]);
    float o[4];
#pragma unroll
    for (int i = 0; i < 4; ++i) {
      const float x = rbf(gs[i]);
      const float y = 0.7978845608028654f * (x + 0.044715f * x * x * x);
      const float ge = x * __builtin_amdgcn_rcpf(1.f + __builtin_amdgcn_exp2f(-2.885390081777927f * y));
      o[i] = rbf(ge) * rbf(us[i]);
    }
    ((LAS v2u*)ax)[((hw * 4 + g) * 8 + rb) * 2 + up] = v2u{pk(o[0], o[1]), pk(o[2], o[3])};
    BARRIER();
    f32x4 c0 = {0.f, 0.f, 0.f, 0.f}, c1 = c0;
#pragma unroll
    for (int s = 0; s < 4; ++s) {
      const v4u xf = ax[(s * 4 + g) * 8 + rb];
      c0 = mfma(wf[2 * s], xf, c0); c1 = mfma(wf[2 * s + 1], xf, c1);
    }
    sq_store(c0, c1, pa);
  }
  if (reducer) {
    __builtin_amdgcn_s_sleep(NAP5);
    v4u a[4];
    gather_all<4>(a, rws, WS_P2 + ri * 3584 + lane * 16, 1024, tag, lane >= 32, t0);
    f32x4 s = (untag(a[0]) + untag(a[1])) + (untag(a[2]) + untag(a[3]));
#pragma unroll
    for (int i = 0; i < 4; ++i) s[i] = xrow_sum(s[i] + dpp<XOR8>(s[i]));
    const v2u ov = {pk(bflo(hres.x) + rbf(s.x), bfhi(hres.x) + rbf(s.y)), pk(bflo(hres.y) + rbf(s.z), bfhi(hres.y) + rbf(s.w))};
    if (lane < 8) *(GAS v2u*)(p.out + ((ri & 1) * 4 + (rb >> 1)) * D + (ri >> 1) * 8 + (rb & 1) * 4) = ov;
  }
}
}

extern "C" void kernel_launch(void* const* d_in, const int* in_sizes, int n_in, void* d_out, int out_size, void* d_ws, size_t ws_size,
                              hipStream_t stream) {
  Params p;
  p.x = (const u16*)d_in[0]; p.kc = (const u16*)d_in[1]; p.vc = (const u16*)d_in[2]; p.wq = (const u16*)d_in[3]; p.wo = (const u16*)d_in[4];
  p.wg = (const u16*)d_in[6]; p.wu = (const u16*)d_in[7]; p.wd = (const u16*)d_in[8];
  p.out = (u16*)d_out; p.ws = (unsigned char*)d_ws;
  hipLaunchKernelGGL(fused_decode_block, dim3(NWG), dim3(NTHR), 0, stream, p);
}
```
